```python
import torch

def _softmax(x, axis=-1):
    return torch.softmax(x, dim=axis)
def _gelu(x):
    return torch.nn.functional.gelu(x, approximate="tanh")
def _silu(x):
    return torch.nn.functional.silu(x)
def _sigmoid(x):
    return torch.sigmoid(x)
def _layernorm(x, axis=-1, eps=1e-5):
    m = x.mean(dim=axis, keepdim=True)
    v = x.var(dim=axis, keepdim=True, unbiased=False)
    return (x - m) / torch.sqrt(v + eps)
def _scatter_add(tgt, idx, src, axis=0):
    return tgt.clone().index_add_(axis, idx, src)
def _segment_sum(src, seg, n):
    out = torch.zeros((n,) + src.shape[1:], dtype=src.dtype)
    return out.index_add_(0, seg, src)
def _pad(x, pw):
    flat = tuple(v for pair in reversed(pw) for v in pair)
    return torch.nn.functional.pad(x, flat)

kernel_name = 'standalone__molecular_angular_gemnet__graph'

def setup_inputs():

    _w = (lambda a: (lambda t: t.to(torch.bfloat16) if t.is_floating_point() else t)(torch.as_tensor(a)))
    N, deg, De, L = 12500, 32, 4, 1
    E = N * deg
    T = E * deg
    NR = 8
    dist = torch.rand(E).to(float)
    angle = (torch.rand(T) * torch.pi).to(float)
    centers = torch.linspace(0.0, 1.0, NR).to(float)
    ang_freq = torch.arange(1, L + 1).to(float)
    return {
        "m": _w((lambda *s: torch.randn(*s, dtype=torch.bfloat16) * 0.05)(E, De)),
        "idx_in": _w((lambda lo, hi, *s: torch.randint(lo, hi, s))(0, E, T).to(torch.int64)),
        "idx_out": _w((lambda lo, hi, *s: torch.randint(lo, hi, s))(0, E, T).to(torch.int64)),
        "dist": _w(dist),
        "angle": _w(angle),
        "centers": _w(centers),
        "ang_freq": _w(ang_freq),
        "w_rbf": _w((lambda *s: torch.randn(*s, dtype=torch.bfloat16) * 0.4)(NR, De)),
        "w_sbf": _w((lambda *s: torch.randn(*s, dtype=torch.bfloat16) * 0.4)(NR * L, De)),
        "w_down": _w((lambda *s: torch.randn(*s, dtype=torch.bfloat16) * 0.4)(De, De)),
        "w_up": _w((lambda *s: torch.randn(*s, dtype=torch.bfloat16) * 0.4)(De, De)),
    }

def reference(m, idx_in, idx_out, dist, angle, centers, ang_freq, w_rbf, w_sbf, w_down, w_up):
    E, De = m.shape[0], m.shape[1]
    NR = centers.shape[0]
    L = ang_freq.shape[0]
    rbf = torch.exp(-10.0 * (dist[:, None] - centers[None, :]) ** 2)
    cbf = torch.cos(ang_freq[None, :] * angle[:, None])
    rbf_t = rbf[idx_out]
    sbf = torch.reshape(rbf_t[:, :, None] * cbf[:, None, :], (-1, NR * L))
    x = (m[idx_in] @ w_down) * (sbf @ w_sbf)
    agg = _segment_sum(x, idx_out, E)
    return m + (agg @ w_up) * (rbf @ w_rbf)
```

```cpp
#include <hip/hip_runtime.h>
#include <hip/hip_bf16.h>
#include <stdint.h>

static __device__ __forceinline__ float bf2f(unsigned short u) { return __uint_as_float(((unsigned)u) << 16); }
static __device__ __forceinline__ float rbf16(float f) { return __bfloat162float(__float2bfloat16(f)); }

__global__ void k_scatter(const uint2* __restrict__ m, const int* __restrict__ idx_in, const int* __restrict__ idx_out,
                          const unsigned short* __restrict__ angle, const unsigned short* __restrict__ freq,
                          float* __restrict__ S, int T) {
    int t = blockIdx.x * blockDim.x + threadIdx.x;
    if (t >= T) return;
    float f = bf2f(freq[0]);
    float c = rbf16(cosf(rbf16(f * bf2f(angle[t]))));
    uint2 v = m[idx_in[t]];
    float* d = S + 4 * (size_t)idx_out[t];
    atomicAdd(d + 0, c * __uint_as_float(v.x << 16));
    atomicAdd(d + 1, c * __uint_as_float(v.x & 0xffff0000u));
    atomicAdd(d + 2, c * __uint_as_float(v.y << 16));
    atomicAdd(d + 3, c * __uint_as_float(v.y & 0xffff0000u));
}

__global__ void k_final(const unsigned short* __restrict__ m, const unsigned short* __restrict__ dist,
                        const unsigned short* __restrict__ centers, const unsigned short* __restrict__ w_rbf,
                        const unsigned short* __restrict__ w_sbf, const unsigned short* __restrict__ w_down,
                        const unsigned short* __restrict__ w_up, const float* __restrict__ S,
                        __hip_bfloat16* __restrict__ out, int E) {
    int e = blockIdx.x * blockDim.x + threadIdx.x;
    if (e >= E) return;
    float d = bf2f(dist[e]);
    float g[4] = {0, 0, 0, 0}, h[4] = {0, 0, 0, 0};
    for (int r = 0; r < 8; r++) {
        float x = rbf16(d - bf2f(centers[r]));
        x = rbf16(x * x);
        x = rbf16(-10.0f * x);
        x = rbf16(expf(x));
        for (int k = 0; k < 4; k++) {
            g[k] += x * bf2f(w_sbf[r * 4 + k]);
            h[k] += x * bf2f(w_rbf[r * 4 + k]);
        }
    }
    float s[4];
    for (int k = 0; k < 4; k++) s[k] = S[4 * e + k];
    float agg[4];
    for (int k = 0; k < 4; k++) {
        float a = 0;
        for (int j = 0; j < 4; j++) a += s[j] * bf2f(w_down[j * 4 + k]);
        agg[k] = a * g[k];
    }
    for (int k = 0; k < 4; k++) {
        float a = 0;
        for (int j = 0; j < 4; j++) a += agg[j] * bf2f(w_up[j * 4 + k]);
        out[4 * e + k] = __float2bfloat16(bf2f(m[4 * e + k]) + a * h[k]);
    }
}

extern "C" void kernel_launch(void* const* d_in, const int* in_sizes, int n_in,
                              void* d_out, int out_size, void* d_ws, size_t ws_size,
                              hipStream_t stream) {
    int E = in_sizes[3];
    int T = in_sizes[1];
    float* S = (float*)d_ws;
    hipMemsetAsync(S, 0, (size_t)E * 16, stream);
    k_scatter<<<(T + 255) / 256, 256, 0, stream>>>((const uint2*)d_in[0], (const int*)d_in[1], (const int*)d_in[2],
        (const unsigned short*)d_in[4], (const unsigned short*)d_in[6], S, T);
    k_final<<<(E + 255) / 256, 256, 0, stream>>>((const unsigned short*)d_in[0], (const unsigned short*)d_in[3],
        (const unsigned short*)d_in[5], (const unsigned short*)d_in[7], (const unsigned short*)d_in[8],
        (const unsigned short*)d_in[9], (const unsigned short*)d_in[10], S, (__hip_bfloat16*)d_out, E);
}
```

```cpp
#define K1_TM 7
#include <hip/hip_runtime.h>
#include <hip/hip_bf16.h>
#include <stdint.h>

typedef unsigned int u32;
typedef unsigned short u16;
typedef unsigned long long u64;
typedef __attribute__((ext_vector_type(4))) u32 u32x4;
typedef __attribute__((ext_vector_type(3))) u32 u32x3;
typedef __attribute__((ext_vector_type(2))) u32 u32x2;
typedef __attribute__((ext_vector_type(2))) float f32x2;
typedef __attribute__((ext_vector_type(4))) float f32x4;

#pragma clang diagnostic ignored "-Wint-to-pointer-cast"
#define DEVI static __device__ __forceinline__

DEVI float bf2f(u32 u) { return __uint_as_float(u << 16); }
DEVI float rbf16(float f) { return __bfloat162float(__float2bfloat16(f)); }

__device__ const float kRevTab[112][2] = {
    {0x1.f304000000000p-2f, 0x1.6e4e440000000p-17f},
    {0x1.f306000000000p-1f, 0x1.b939100000000p-18f},
    {0x1.e60c000000000p-1f, 0x1.b939100000000p-17f},
    {0x1.cc1a000000000p-1f, 0x1.7272200000000p-17f},
    {0x1.9836000000000p-1f, 0x1.c9c8820000000p-18f},
    {0x1.306c000000000p-1f, 0x1.c9c8820000000p-17f},
    {0x1.8368000000000p-3f, 0x1.9391060000000p-17f},
    {0x1.836c000000000p-2f, 0x1.27220a0000000p-17f},
    {0x1.836e000000000p-1f, 0x1.3910540000000p-19f},
    {0x1.06dc000000000p-1f, 0x1.3910540000000p-18f},
    {0x1.b700000000000p-6f, 0x1.3910540000000p-17f},
    {0x1.b720000000000p-5f, 0x1.c882a60000000p-19f},
    {0x1.b720000000000p-4f, 0x1.c882a60000000p-18f},
    {0x1.b720000000000p-3f, 0x1.c882a60000000p-17f},
    {0x1.b724000000000p-2f, 0x1.91054a0000000p-17f},
    {0x1.b726000000000p-1f, 0x1.220a940000000p-17f},
    {0x1.6e4e000000000p-1f, 0x1.1054a80000000p-19f},
    {0x1.b938000000000p-2f, 0x1.1054a80000000p-18f},
    {0x1.b938000000000p-1f, 0x1.1054a80000000p-17f},
    {0x1.7272000000000p-1f, 0x1.054a800000000p-20f},
    {0x1.c9c8000000000p-2f, 0x1.054a800000000p-19f},
    {0x1.c9c8000000000p-1f, 0x1.054a800000000p-18f},
    {0x1.9390000000000p-1f, 0x1.054a800000000p-17f},
    {0x1.2722000000000p-1f, 0x1.529fc20000000p-22f},
    {0x1.3910000000000p-3f, 0x1.529fc20000000p-21f},
    {0x1.3910000000000p-2f, 0x1.529fc20000000p-20f},
    {0x1.3910000000000p-1f, 0x1.529fc20000000p-19f},
    {0x1.c880000000000p-3f, 0x1.529fc20000000p-18f},
    {0x1.c880000000000p-2f, 0x1.529fc20000000p-17f},
    {0x1.c882000000000p-1f, 0x1.4a7f0a0000000p-18f},
    {0x1.9104000000000p-1f, 0x1.4a7f0a0000000p-17f},
    {0x1.220a000000000p-1f, 0x1.29fc280000000p-18f},
    {0x1.1050000000000p-3f, 0x1.29fc280000000p-17f},
    {0x1.1054000000000p-2f, 0x1.4fe13a0000000p-19f},
    {0x1.1054000000000p-1f, 0x1.4fe13a0000000p-18f},
    {0x1.0540000000000p-4f, 0x1.4fe13a0000000p-17f},
    {0x1.0548000000000p-3f, 0x1.3f84ea0000000p-18f},
    {0x1.0548000000000p-2f, 0x1.3f84ea0000000p-17f},
    {0x1.054a000000000p-1f, 0x1.fc27580000000p-19f},
    {0x1.5280000000000p-6f, 0x1.fc27580000000p-18f},
    {0x1.5280000000000p-5f, 0x1.fc27580000000p-17f},
    {0x1.5290000000000p-4f, 0x1.f84eb00000000p-17f},
    {0x1.5298000000000p-3f, 0x1.f09d600000000p-17f},
    {0x1.529c000000000p-2f, 0x1.e13abe0000000p-17f},
    {0x1.529e000000000p-1f, 0x1.c2757e0000000p-17f},
    {0x1.4a7c000000000p-2f, 0x1.84eafa0000000p-17f},
    {0x1.4a7e000000000p-1f, 0x1.09d5f40000000p-17f},
    {0x1.29fc000000000p-2f, 0x1.3abe900000000p-21f},
    {0x1.29fc000000000p-1f, 0x1.3abe900000000p-20f},
    {0x1.4fe0000000000p-3f, 0x1.3abe900000000p-19f},
    {0x1.4fe0000000000p-2f, 0x1.3abe900000000p-18f},
    {0x1.4fe0000000000p-1f, 0x1.3abe900000000p-17f},
    {0x1.3f84000000000p-2f, 0x1.d5f47e0000000p-19f},
    {0x1.3f84000000000p-1f, 0x1.d5f47e0000000p-18f},
    {0x1.fc20000000000p-3f, 0x1.d5f47e0000000p-17f},
    {0x1.fc24000000000p-2f, 0x1.abe8fa0000000p-17f},
    {0x1.fc26000000000p-1f, 0x1.57d1f60000000p-17f},
    {0x1.f84e000000000p-1f, 0x1.5f47d40000000p-18f},
    {0x1.f09c000000000p-1f, 0x1.5f47d40000000p-17f},
    {0x1.e13a000000000p-1f, 0x1.7d1f540000000p-18f},
    {0x1.c274000000000p-1f, 0x1.7d1f540000000p-17f},
    {0x1.84ea000000000p-1f, 0x1.f47d4e0000000p-18f},
    {0x1.09d4000000000p-1f, 0x1.f47d4e0000000p-17f},
    {0x1.3aa0000000000p-5f, 0x1.e8fa9a0000000p-17f},
    {0x1.3ab0000000000p-4f, 0x1.d1f5340000000p-17f},
    {0x1.3ab8000000000p-3f, 0x1.a3ea6a0000000p-17f},
    {0x1.3abc000000000p-2f, 0x1.47d4d40000000p-17f},
    {0x1.3abe000000000p-1f, 0x1.1f534e0000000p-18f},
    {0x1.d5f0000000000p-3f, 0x1.1f534e0000000p-17f},
    {0x1.d5f4000000000p-2f, 0x1.f534de0000000p-20f},
    {0x1.d5f4000000000p-1f, 0x1.f534de0000000p-19f},
    {0x1.abe8000000000p-1f, 0x1.f534de0000000p-18f},
    {0x1.57d0000000000p-1f, 0x1.f534de0000000p-17f},
    {0x1.5f44000000000p-2f, 0x1.ea69bc0000000p-17f},
    {0x1.5f46000000000p-1f, 0x1.d4d3780000000p-17f},
    {0x1.7d1c000000000p-2f, 0x1.a9a6ee0000000p-17f},
    {0x1.7d1e000000000p-1f, 0x1.534ddc0000000p-17f},
    {0x1.f47c000000000p-2f, 0x1.4d37700000000p-18f},
    {0x1.f47c000000000p-1f, 0x1.4d37700000000p-17f},
    {0x1.e8fa000000000p-1f, 0x1.34ddc00000000p-18f},
    {0x1.d1f4000000000p-1f, 0x1.34ddc00000000p-17f},
    {0x1.a3ea000000000p-1f, 0x1.a6ee060000000p-19f},
    {0x1.47d4000000000p-1f, 0x1.a6ee060000000p-18f},
    {0x1.1f50000000000p-2f, 0x1.a6ee060000000p-17f},
    {0x1.1f52000000000p-1f, 0x1.4ddc0e0000000p-17f},
    {0x1.f530000000000p-4f, 0x1.3770360000000p-18f},
    {0x1.f530000000000p-3f, 0x1.3770360000000p-17f},
    {0x1.f534000000000p-2f, 0x1.bb81b60000000p-19f},
    {0x1.f534000000000p-1f, 0x1.bb81b60000000p-18f},
    {0x1.ea68000000000p-1f, 0x1.bb81b60000000p-17f},
    {0x1.d4d2000000000p-1f, 0x1.77036e0000000p-17f},
    {0x1.a9a6000000000p-1f, 0x1.dc0db60000000p-18f},
    {0x1.534c000000000p-1f, 0x1.dc0db60000000p-17f},
    {0x1.4d34000000000p-2f, 0x1.b81b6c0000000p-17f},
    {0x1.4d36000000000p-1f, 0x1.7036d80000000p-17f},
    {0x1.34dc000000000p-2f, 0x1.c0db620000000p-18f},
    {0x1.34dc000000000p-1f, 0x1.c0db620000000p-17f},
    {0x1.a6e8000000000p-3f, 0x1.81b6c60000000p-17f},
    {0x1.a6ec000000000p-2f, 0x1.036d8a0000000p-17f},
    {0x1.a6ee000000000p-1f, 0x1.b6c52c0000000p-23f},
    {0x1.4ddc000000000p-1f, 0x1.b6c52c0000000p-22f},
    {0x1.3770000000000p-2f, 0x1.b6c52c0000000p-21f},
    {0x1.3770000000000p-1f, 0x1.b6c52c0000000p-20f},
    {0x1.bb80000000000p-3f, 0x1.b6c52c0000000p-19f},
    {0x1.bb80000000000p-2f, 0x1.b6c52c0000000p-18f},
    {0x1.bb80000000000p-1f, 0x1.b6c52c0000000p-17f},
    {0x1.7702000000000p-1f, 0x1.6d8a560000000p-17f},
    {0x1.dc0c000000000p-2f, 0x1.b6295a0000000p-18f},
    {0x1.dc0c000000000p-1f, 0x1.b6295a0000000p-17f},
    {0x1.b81a000000000p-1f, 0x1.6c52b40000000p-17f},
    {0x1.7036000000000p-1f, 0x1.b14acc0000000p-18f},
    {0x1.c0d8000000000p-2f, 0x1.b14acc0000000p-17f}
};

#define K1_TS 1
constexpr u32 RO = 6250;
constexpr u32 RI = 7144;
constexpr u32 NST = 7;
constexpr u32 NBINS = 256 * NST;
constexpr u32 CH_CAP = 25600;
constexpr u32 QFULL = 6144;
constexpr u32 OFF_STRIDE = 4096;
constexpr u32 CHB = 137088;
constexpr u32 NWG = 256;
constexpr u32 QP = (RO / 2 + 3) / 4;

typedef __amdgpu_buffer_rsrc_t rsrc_t;
DEVI rsrc_t make_rsrc(const void* p, u32 bytes) { return __builtin_amdgcn_make_buffer_rsrc((void*)p, 0, bytes, 0x00020000); }
constexpr u32 OOB = 0x80000000u;

typedef __attribute__((ext_vector_type(2))) __bf16 bf16x2;
DEVI u32 mulhi24(u32 a, u32 b) { return (u32)(((u64)(a & 0xffffffu) * (u64)(b & 0xffffffu)) >> 32); }
DEVI u32 cvt_pk_bf16(f32x2 v) { return __builtin_bit_cast(u32, __builtin_convertvector(v, bf16x2)); }
DEVI u32 cvt_pk_bf16(float lo, float hi) { return cvt_pk_bf16((f32x2){lo, hi}); }
DEVI f32x2 unpack_bf16(u32 v) { return (f32x2){__uint_as_float(v << 16), __uint_as_float(v & 0xffff0000u)}; }
DEVI u32 mad24(u32 a, u32 b, u32 c) { u32 r; asm("v_mad_i32_i24 %0, %1, %2, %3" : "=v"(r) : "v"(a), "s"(b), "v"(c)); return r; }
DEVI u32 opaque(u32 x) { asm volatile("" : "+v"(x)); return x; }

DEVI float rev_big(float x) {
    u32 b = __float_as_uint(x);
    u32 e = (b >> 23) & 0xff;
    if (e == 255) return __uint_as_float(0x7fc00000u);
    float k = x, hi = 0x1.45f2p-3f, lo = 0x1.06dc9cp-19f;
    if (e >= 143) {
        k = __uint_as_float((b & 0x007f0000u) | 0x43000000u);
        hi = kRevTab[e - 143][0]; lo = kRevTab[e - 143][1];
    }
    return __builtin_fmaf(k, lo, __builtin_amdgcn_fractf(k * hi));
}

template <int CTRL, int RM, int BM>
DEVI u32 dpp0(u32 v) { return __builtin_amdgcn_update_dpp(0u, v, CTRL, RM, BM, false); }
DEVI u32 wave_incl_add(u32 v) {
    v += dpp0<0x111, 0xf, 0xf>(v);
    v += dpp0<0x112, 0xf, 0xf>(v);
    v += dpp0<0x114, 0xf, 0xf>(v);
    v += dpp0<0x118, 0xf, 0xf>(v);
    v += dpp0<0x142, 0xa, 0xf>(v);
    v += dpp0<0x143, 0xc, 0xf>(v);
    return v;
}
DEVI u32 half_incl_add(u32 v) {
    v += dpp0<0x111, 0xf, 0xf>(v);
    v += dpp0<0x112, 0xf, 0xf>(v);
    v += dpp0<0x114, 0xf, 0xf>(v);
    v += dpp0<0x118, 0xf, 0xf>(v);
    v += dpp0<0x142, 0xa, 0xf>(v);
    return v;
}
DEVI u32 umax(u32 a, u32 b) { return a > b ? a : b; }
DEVI u32 wave_incl_max(u32 v) {
    v = umax(v, dpp0<0x111, 0xf, 0xf>(v));
    v = umax(v, dpp0<0x112, 0xf, 0xf>(v));
    v = umax(v, dpp0<0x114, 0xf, 0xf>(v));
    v = umax(v, dpp0<0x118, 0xf, 0xf>(v));
    v = umax(v, dpp0<0x142, 0xa, 0xf>(v));
    v = umax(v, dpp0<0x143, 0xc, 0xf>(v));
    return v;
}

#define LDSP(T, off) ((__attribute__((address_space(3))) T*)(off))
DEVI void lds_barrier() { asm volatile("s_waitcnt lgkmcnt(0)\n\ts_barrier" ::: "memory"); }
#define CBAR() asm volatile("" ::: "memory")
#define SB() __builtin_amdgcn_sched_barrier(0)
template <int N> struct IntC { static constexpr int value = N; };
#define NEW_PAGE(n) asm volatile("s_branch 1f\n\t.p2align 12\nkp_" #n ":\n\ts_setpc_b64 vcc\n1:" ::: "memory")
#define TOUCH(k, l) "s_cmp_eq_u32 %0, " #k "\n\ts_cbranch_scc1 " l "\n\t"
DEVI void touch_pages(u32 wave) {
    asm volatile("s_getpc_b64 vcc\n2:\n\ts_add_u32 vcc_lo, vcc_lo, 3f-2b\n\ts_addc_u32 vcc_hi, vcc_hi, 0\n\t"
                 TOUCH(4, "kw1_0") TOUCH(5, "kw1_1") TOUCH(6, "kw1_2") TOUCH(7, "kw1_3") TOUCH(8, "kw1_4")
                 TOUCH(9, "kp_0") TOUCH(10, "kp_1") TOUCH(11, "kp_2") "3:" :: "s"(wave) : "vcc", "scc", "memory");
}

constexpr u32 K1_HIST = 0;
constexpr u32 K1_WSUM = NBINS * 8;
constexpr u32 K1_WMAX = K1_WSUM + 64;
constexpr u32 K1_GO = K1_WMAX + 64;
constexpr u32 K1_REC = K1_WMAX + 64 + 16;
constexpr u32 K1_LDS = 163840;
constexpr u32 K2_MISC = K1_LDS - 96;
static_assert(K1_REC % 16 == 0 && (CH_CAP + NBINS) * 5 <= CHB && CHB <= 9 * 16384 && K1_REC + 9 * 16384 <= K2_MISC, "lds");

struct Raw { u32x4 io, ii; u32x2 an; };
struct K1Ctx { float freq; bool one; };
constexpr u32 MAGIC_O = (u32)(((1ull << 32) + RO - 1) / RO), MAGIC_I = (u32)(((1ull << 32) + RI - 1) / RI);
constexpr u32 H_NONE = 0xffffffffu;

DEVI void make_rec(u32 io, u32 ii, u32 ctop, const K1Ctx& c, bool valid, u32& w, u32& h) {
    u32 o = mulhi24(io, MAGIC_O), b = mulhi24(ii, MAGIC_I);
    h = mad24(o, (NST * 16) << 16, mad24(b, 131072u - 8 * RI, ii << 3));
    w = mad24(o, 0u - 64 * RO, (io << 6) + ctop) + __builtin_amdgcn_ubfe(h, 3, 5) + 64;
    w = (w & ~0x40000000u) | ((b << 30) & 0x40000000u);
    if (!valid) h = H_NONE;
    asm volatile("" : "+v"(w), "+v"(h));
}
DEVI u32 bin_addr(u32 h) { return K1_HIST + ((h >> 16) & 0xfffcu); }
template <u32 HO> DEVI void hist_add(u32 h) {
    __hip_atomic_fetch_add(LDSP(u32, HO + bin_addr(h)), 1u, __ATOMIC_RELAXED, __HIP_MEMORY_SCOPE_WORKGROUP);
}
template <u32 HO> DEVI u32 cursor_add(u32 h) {
    return __hip_atomic_fetch_add(LDSP(u32, HO + bin_addr(h)), 5u, __ATOMIC_RELAXED, __HIP_MEMORY_SCOPE_WORKGROUP);
}
DEVI void put_rec(u32 y, u32 w, u32 h) {
    const u32 a = y & 0x3fffcu, odd = y & 1u;
    *LDSP(u32, K1_REC + a) = w | (odd << 5);
    *LDSP(unsigned char, K1_REC - 2 + mad24(y & 2u, 5u, mad24(odd, 0u - 3u, a))) = (unsigned char)(h >> 8);
}
DEVI u32 squeeze(u32 c) { return (c & 0x80008000u) | ((c << 3) & 0x7fff7fffu); }
DEVI void unpack_rec(u32 w, u32 byte, float& cs, u32& row1, u32& in_local) {
    cs = __uint_as_float((((u32)((int)w >> 3)) & 0x87ff0000u) | 0x38000000u);
    row1 = (w >> 6) & 0x1fffu;
    in_local = (byte << 5) | (w & 31u);
}

DEVI void cos4(u32x2 an, const K1Ctx& c, u32& c01, u32& c23, u32& nanacc) {
    u32 x01 = an.x, x23 = an.y;
    if (!c.one) {
        x01 = cvt_pk_bf16(unpack_bf16(an.x) * c.freq);
        x23 = cvt_pk_bf16(unpack_bf16(an.y) * c.freq);
        asm volatile("" : "+v"(x01), "+v"(x23));
    }
    f32x2 xa = unpack_bf16(x01), xb = unpack_bf16(x23);
    f32x2 ra = xa * 0x1.45f306p-3f, rb = xb * 0x1.45f306p-3f;
    u32 big = ((x01 & 0x7fff7fffu) + 0x3e803e80u) | ((x23 & 0x7fff7fffu) + 0x3e803e80u);
    if (__builtin_expect(__any((big & 0x80008000u) != 0), 0)) {
        if (!(__builtin_fabsf(xa.x) < 16.f)) ra.x = rev_big(xa.x);
        if (!(__builtin_fabsf(xa.y) < 16.f)) ra.y = rev_big(xa.y);
        if (!(__builtin_fabsf(xb.x) < 16.f)) rb.x = rev_big(xb.x);
        if (!(__builtin_fabsf(xb.y) < 16.f)) rb.y = rev_big(xb.y);
        if (ra.x != ra.x || ra.y != ra.y || rb.x != rb.x || rb.y != rb.y) nanacc = 0x7fffu;
    }
    c01 = squeeze(cvt_pk_bf16(__builtin_amdgcn_cosf(ra.x), __builtin_amdgcn_cosf(ra.y)));
    c23 = squeeze(cvt_pk_bf16(__builtin_amdgcn_cosf(rb.x), __builtin_amdgcn_cosf(rb.y)));
}

DEVI u32 cos1(u32 an, const K1Ctx& c, u32& nanacc) {
    float x = __uint_as_float(cvt_pk_bf16(0.f, bf2f(an) * c.freq));
    float r = x * 0x1.45f306p-3f;
    if (__builtin_expect(!(__builtin_fabsf(x) < 16.f), 0)) {
        r = rev_big(x);
        if (r != r) nanacc = 0x7fffu;
    }
    return squeeze(cvt_pk_bf16(0.f, __builtin_amdgcn_cosf(r)));
}

constexpr int AUX_NT = 2, AUX_SC1 = 16;

struct Src { rsrc_t io, ii, an; u32 piece; };
template <bool FULL>
DEVI void load_quad(Raw& r, const Src& s, u32 tid, u32 slot) {
    if (FULL) {
        const u32 p = s.piece + slot * NWG;
        r.io = __builtin_amdgcn_raw_buffer_load_b128(s.io, tid * 16, p * 16384, AUX_NT);
        r.ii = __builtin_amdgcn_raw_buffer_load_b128(s.ii, tid * 16, p * 16384, AUX_NT);
        r.an = __builtin_amdgcn_raw_buffer_load_b64(s.an, tid * 8, p * 8192, AUX_NT);
    } else {
        u32 q = tid + slot * 1024;
        r.io = __builtin_amdgcn_raw_buffer_load_b128(s.io, q * 16, 0, AUX_NT);
        r.ii = __builtin_amdgcn_raw_buffer_load_b128(s.ii, q * 16, 0, AUX_NT);
        r.an = __builtin_amdgcn_raw_buffer_load_b64(s.an, q * 8, 0, AUX_NT);
    }
}
template <bool FULL, u32 HO = 0>
DEVI void proc_quad(const Raw& r, const K1Ctx& c, u32 q, u32 nq, u32* w, u32* h, u32& nanacc) {
    u32 c01, c23;
    cos4(r.an, c, c01, c23, nanacc);
    bool valid = FULL || q < nq;
    make_rec(r.io.x, r.ii.x, c01 << 16, c, valid, w[0], h[0]);
    make_rec(r.io.y, r.ii.y, c01 & 0xfff80000u, c, valid, w[1], h[1]);
    make_rec(r.io.z, r.ii.z, c23 << 16, c, valid, w[2], h[2]);
    make_rec(r.io.w, r.ii.w, c23 & 0xfff80000u, c, valid, w[3], h[3]);
    if (valid) { hist_add<HO>(h[0]); hist_add<HO>(h[1]); hist_add<HO>(h[2]); hist_add<HO>(h[3]); }
}
template <bool FULL, u32 HO = 0>
DEVI void scatter_quad(const u32* w, u32* h) {
    asm volatile("" : "+v"(h[0]), "+v"(h[1]), "+v"(h[2]), "+v"(h[3]));
    if (!FULL && h[0] == H_NONE) return;
    u32 r0 = cursor_add<HO>(h[0]), r1 = cursor_add<HO>(h[1]), r2 = cursor_add<HO>(h[2]), r3 = cursor_add<HO>(h[3]);
    put_rec(r0, w[0], h[0]); put_rec(r1, w[1], h[1]); put_rec(r2, w[2], h[2]); put_rec(r3, w[3], h[3]);
}

constexpr u32 K2_MBLK = 0;
constexpr u32 K2_EXP = RI * 8;
constexpr u32 K2_ACC0 = K2_EXP + 16 * 320;
constexpr u32 K2_ACCA = K2_ACC0 + 16;
constexpr u32 K2_WFR = K2_ACCA + RO * 16;
static_assert(K2_ACCA % 16 == 0 && K2_WFR + 1024 <= K2_MISC, "lds");

struct Args {
    const int* idx_in; const int* idx_out; const u16* angle; u32 CH, E;
    const u16* freq; const u32x2* m2; unsigned char* ws; u32* offs;
    const u16* dist; const u32* centers; const u32* w_down; const u32* w_up;
    u32x2* out; const u16* w_sbf; const u16* w_rbf; u32 T, nchunks;
};
static_assert(sizeof(Args) == 128, "no padding");
constexpr u32 NREP = 16;
constexpr u32 WS_DONE = 0;
constexpr u32 WS_FLAG = NREP * 1024;
constexpr u32 WS_ASK = 2 * NREP * 1024;
constexpr u32 WS_ECHO = WS_ASK + 1024;
constexpr u32 WS_ARR = WS_ECHO + 1024;
constexpr u32 WS_PART = WS_ARR + 64 * 256;
constexpr size_t WS_REC = (WS_PART + (size_t)NWG * RO * 24 + 255) & ~(size_t)255;

DEVI u32 ld_agent(const u32* p) { return __hip_atomic_load(p, __ATOMIC_RELAXED, __HIP_MEMORY_SCOPE_AGENT); }
DEVI void st_agent(u32* p, u32 v) { __hip_atomic_store(p, v, __ATOMIC_RELAXED, __HIP_MEMORY_SCOPE_AGENT); }

DEVI u16 load_wfrag(const Args& A, u32 tid) {
    const u32 l = tid >> 3;
    u16 v = 0;
    if (tid < 512 && (l & 16) == 0 && ((l >> 2) & 1) == (l >> 5)) v = ((l & 8) ? A.w_rbf : A.w_sbf)[(tid & 7) * 4 + (l & 3)];
    return v;
}
typedef const __attribute__((address_space(4))) u32* cptr;
constexpr u32 NP = RO / 2;
struct Rbf { u32x4 r0, r1; };
DEVI u32 own_pair(u32 tid) {
    const u32 bucket = (blockIdx.x & 7) * 32 + (blockIdx.x >> 3), g = bucket >> 6, o = bucket & 63;
    return tid < (g == 3 ? NP - 3 * QP : QP) ? o * NP + g * QP + tid : 0x08000000u;
}
DEVI u32x4 rbf_row(float d, cptr centers) {
    u32x4 rb;
#pragma unroll
    for (int r = 0; r < 4; r++) {
        f32x2 x = unpack_bf16(cvt_pk_bf16((f32x2){d, d} - unpack_bf16(centers[r])));
        x = unpack_bf16(cvt_pk_bf16(x * x));
        x = unpack_bf16(cvt_pk_bf16(x * -10.0f));
        x = x * 0x1.715476p+0f;
        rb[r] = cvt_pk_bf16(__builtin_amdgcn_exp2f(x.x), __builtin_amdgcn_exp2f(x.y));
    }
    asm volatile("" : "+v"(rb));
    return rb;
}
DEVI u32 tab_index(u32 chunk, u32 bucket, u32 st) {
    return ((chunk >> 9) * NWG + (chunk & (NWG - 1))) * OFF_STRIDE + bucket * 16 + (st >> 1) * 4 + ((chunk >> 8) & 1) * 2 + (st & 1);
}
DEVI void prep_lds(u16 wf, u32 tid, bool zero_acc) {
    u32x4 z = {0u, 0u, 0u, 0u};
    asm volatile("" : "+v"(z));
    if (zero_acc) {
        const u32 a0 = opaque(K2_ACCA + tid * 16), a1 = opaque(K2_ACCA + 65536 + tid * 16);
#pragma unroll
        for (int j = 0; j < 4; j++) *LDSP(u32x4, a0 + j * 16384) = z;
        *LDSP(u32x4, a1) = z; *LDSP(u32x4, a1 + 16384) = z;
        if (tid < RO - 6144) *LDSP(u32x4, a1 + 32768) = z;
    }
    if ((tid & 63) < 20) *LDSP(u32x4, K2_EXP + (tid >> 6) * 320 + (tid & 63) * 16) = z;
    if (tid < 512) *LDSP(u16, K2_WFR + tid * 2) = wf;
}

template <bool FULL>
DEVI u32 sort_phase(const Args& A, Rbf& rbf) {
    const int* const idx_in = A.idx_in; const int* const idx_out = A.idx_out; const u16* const angle = A.angle;
    const u32 CH = A.CH, E = A.E, T = A.T, nchunks = A.nchunks;
    constexpr int ST_AUX = FULL ? AUX_SC1 : 0;
    const u32 tid = threadIdx.x, lane = tid & 63;
    const u32 wave = __builtin_amdgcn_readfirstlane(tid >> 6);
    constexpr u32 H2 = NBINS * 4;
    u32 chunk = blockIdx.x;
    Raw r[6]; u32 sio = 0, sii = 0; u16 san = 0;
    u32 n = 0, nq = 0;
    Src src;
    auto setup = [&](u32 ck) __attribute__((always_inline)) {
        u32 base = FULL ? 0 : ck * CH;
        n = FULL ? CH * (2 * NWG) : (ck < nchunks ? (T - base < CH ? T - base : CH) : 0);
        if (!FULL && ck >= nchunks) base = 0;
        nq = n / 4 < QFULL ? n / 4 : QFULL;
        src.io = make_rsrc(idx_out + base, n * 4);
        src.ii = make_rsrc(idx_in + base, n * 4);
        src.an = make_rsrc(angle + base, n * 2);
        src.piece = blockIdx.x;
    };
    const u32 nsingle = 2 * CH - 12 * 4096;
    auto load_single = [&]() __attribute__((always_inline)) {
        u32 i = (FULL ? 12 * 4096 * NWG + blockIdx.x * nsingle : nq * 4) + opaque(tid);
        if (FULL && threadIdx.x >= nsingle) i = 0x10000000u;
        sio = __builtin_amdgcn_raw_buffer_load_b32(src.io, i * 4, 0, AUX_NT);
        sii = __builtin_amdgcn_raw_buffer_load_b32(src.ii, i * 4, 0, AUX_NT);
        san = __builtin_amdgcn_raw_buffer_load_b16(src.an, i * 2, 0, AUX_NT);
    };
    setup(chunk);
    load_quad<FULL>(r[0], src, tid, 0);
    SB();
    u32* const ask = (u32*)(A.ws + WS_ASK); u32* const echo = (u32*)(A.ws + WS_ECHO); u32* const flags = (u32*)(A.ws + WS_FLAG);
    u32 chal = 0;
    if (FULL && wave == 0) chal = ld_agent(echo + blockIdx.x);
    const rsrc_t rs_sync = make_rsrc(A.ws, WS_ARR);
    if (blockIdx.x == 0) {
        if (FULL) {
            __builtin_amdgcn_raw_buffer_store_b128((u32x4){0u, 0u, 0u, 0u}, rs_sync, WS_DONE + tid * 16, 0, AUX_SC1);
            __builtin_amdgcn_raw_buffer_store_b128((u32x4){0u, 0u, 0u, 0u}, rs_sync, WS_FLAG + tid * 16, 0, AUX_SC1);
        }
        if (tid < 64) st_agent((u32*)(A.ws + WS_ARR) + tid * 64, 0u);
    }
    u16 d0 = 0, d1 = 0;
    if (FULL) {
        const rsrc_t rs_d = make_rsrc(A.dist, E * 2);
        d0 = __builtin_amdgcn_raw_buffer_load_b16(rs_d, own_pair(tid) * 4, 0, 0);
        d1 = __builtin_amdgcn_raw_buffer_load_b16(rs_d, own_pair(tid) * 4 + 2, 0, 0);
    }
    const u32 fq = A.freq[0];
    const u32x4 mv = __builtin_amdgcn_raw_buffer_load_b128(make_rsrc(A.m2, E * 8), (blockIdx.x * 1024 + tid) * 16, 0, 0);
    SB();
    if (tid < NBINS / 2) *LDSP(u32x4, K1_HIST + tid * 16) = (u32x4){0u, 0u, 0u, 0u};
    lds_barrier();
    if (FULL) touch_pages(wave);
    u32 tq[8] = {0, 0, 0, 0, 0, 0, 0, 0};
    if (FULL && (wave & (K1_TS - 1)) == 0) {
        const u32 id = (blockIdx.x >> 3) * 16 + wave;
        if (K1_TM & 1) {
            tq[0] = *(cptr)(idx_in + (size_t)id * CH);
            tq[1] = *(cptr)(idx_out + (size_t)id * CH);
            tq[2] = *(cptr)(angle + (size_t)id * CH);
        }
        if (K1_TM & 2) tq[3] = *(cptr)(A.ws + WS_REC + (size_t)(id + 1) * CHB - 128);
        if (K1_TM & 4) {
            tq[4] = *(cptr)(A.ws + WS_PART + (size_t)(id >> 1) * (RO * 8) + ((id >> 1) & 3) * (QP * 16) + 1024);
            const u32 orow = own_pair(0) * 2 < E ? own_pair(0) * 2 : 0;
            tq[5] = wave == 0 ? *(cptr)(A.offs + (size_t)blockIdx.x * OFF_STRIDE)
                  : wave == 1 && orow ? *(cptr)(A.out + orow) : *(cptr)(A.m2 + (size_t)id * E / 512);
        }
        if (K1_TM & 8) {
            tq[6] = *(cptr)(idx_in + (size_t)id * CH + CH / 2);
            tq[7] = *(cptr)(idx_out + (size_t)id * CH + CH / 2);
        }
    }
    SB();
    if (FULL) __builtin_amdgcn_s_setprio(3);
#pragma unroll
    for (int i = 1; i < (FULL ? 2 : 6); i++) { load_quad<FULL>(r[i], src, tid, i); SB(); }
    if (!FULL) load_single();
    SB();
    if (FULL && wave == 0) {
        chal += 1;
        if (lane == 0) st_agent(ask + blockIdx.x, chal);
    }
    if (FULL) { rbf.r0 = rbf_row(bf2f(d0), (cptr)A.centers); rbf.r1 = rbf_row(bf2f(d1), (cptr)A.centers); }
    SB();
    K1Ctx c; c.freq = bf2f(fq); c.one = __builtin_amdgcn_readfirstlane(fq) == 0x3f80u;
    u32 nanacc = umax(umax(umax(mv.x & 0x7fffu, (mv.x >> 16) & 0x7fffu), umax(mv.y & 0x7fffu, (mv.y >> 16) & 0x7fffu)),
                      umax(umax(mv.z & 0x7fffu, (mv.z >> 16) & 0x7fffu), umax(mv.w & 0x7fffu, (mv.w >> 16) & 0x7fffu)));

    u32 w[24], h[24], ws1, hs1;
    bool sv = false;
    auto hist_quads = [&](auto HO, u32 cnq) __attribute__((always_inline)) {
#pragma unroll
        for (int i = 0; i < 6; i++) proc_quad<FULL, decltype(HO)::value>(r[i], c, tid + i * 1024, cnq, w + 4 * i, h + 4 * i, nanacc);
    };
    auto hist_single = [&](bool valid) __attribute__((always_inline)) {
        sv = valid;
        make_rec(sio, sii, cos1(san, c, nanacc), c, true, ws1, hs1);
        if (sv) hist_add<0>(hs1);
    };
    u32 cnt2;
    u32 hoff = 0;
    auto tab_addr = [&](u32 tid) __attribute__((always_inline)) -> u32 {
        return hoff + K1_HIST + ((tid >> 2) & 63) * (NST * 16) + (tid >> 8) * (NST * 4) + (tid & 3) * 8;
    };
    auto scan = [&]() __attribute__((always_inline)) -> u32 {
        const u32 tid = opaque(threadIdx.x), lane = tid & 63;
        const u32 xs = tid >> 4, x = (xs * 9363u) >> 16, st = xs - x * NST;
        const u32 ca = hoff + K1_HIST + ((x & 1) * 32 + (tid & 15) * 2) * (NST * 16) + ((x >> 1) * NST + st) * 4;
        const bool on = tid < NBINS / 2;
        u32 c0 = 0, c1 = 0;
        if (on) { c0 = *LDSP(u32, ca); c1 = *LDSP(u32, ca + NST * 16); }
        const u32 ta = tab_addr(tid);
        cnt2 = *LDSP(u32, ta) | (*LDSP(u32, ta + 4) << 16);
        const u32 v0 = (c0 + 1) & ~1u, v1 = (c1 + 1) & ~1u;
        const u32 tot = v0 + v1;
        const u32 inc = wave_incl_add(tot);
        if (lane == 63) *LDSP(u32, K1_WSUM + wave * 4) = inc;
        lds_barrier();
        u32 ws = *LDSP(u32, K1_WSUM + (lane & 15) * 4);
        ws += dpp0<0x111, 0xf, 0xf>(ws); ws += dpp0<0x112, 0xf, 0xf>(ws);
        ws += dpp0<0x114, 0xf, 0xf>(ws); ws += dpp0<0x118, 0xf, 0xf>(ws);
        const u32 wbase = wave ? __builtin_amdgcn_readlane(ws, wave - 1) : 0u;
        const u32 e0 = wbase + inc - tot, e1 = e0 + v0;
        if (on) { *LDSP(u32, ca) = (e0 << 17) + e0 * 5 + 2; *LDSP(u32, ca + NST * 16) = (e1 << 17) + e1 * 5 + 2; }
        if (c0 & 1) put_rec((e0 + c0) * 5 + 2, 0u, 0u);
        if (c1 & 1) put_rec((e1 + c1) * 5 + 2, 0u, 0u);
        return __builtin_amdgcn_readlane(ws, 15);
    };
    u32x2 tabv;
    auto get_table = [&]() __attribute__((always_inline)) {
        const u32 tid = opaque(threadIdx.x);
        const u32 ta = tab_addr(tid);
        const u32 s0 = *LDSP(u32, ta) >> 18 << 1, s1 = *LDSP(u32, ta + 4) >> 18 << 1;
        tabv = (u32x2){s0 | (cnt2 << 16), (tid & 3) != 3 ? s1 | (cnt2 & 0xffff0000u) : 0u};
        asm volatile("" : "+v"(tabv));
    };
    auto zero_hist = [&]() __attribute__((always_inline)) {
        const u32 tid = opaque(threadIdx.x);
        if (tid < NBINS / 2) *LDSP(u32x2, K1_HIST + tid * 8) = (u32x2){0u, 0u};
    };
    auto scatter_single = [&]() __attribute__((always_inline)) {
        asm volatile("" : "+v"(hs1));
        if (sv) put_rec(cursor_add<0>(hs1), ws1, hs1);
    };
    u32x4 ob[9];
    u32 olen;
    rsrc_t dst;
    auto read_out = [&](u32 cur, u32 len) __attribute__((always_inline)) {
        olen = ((len + 3) >> 2) * 20;
        dst = make_rsrc(A.ws + WS_REC + (size_t)cur * CHB, olen);
        const u32 t16 = opaque(threadIdx.x) * 16;
        const u32 a0 = opaque(K1_REC + t16), a1 = opaque(K1_REC + 65536 + t16), a2 = opaque(K1_REC + 131072 + t16);
#pragma unroll
        for (int i = 0; i < 9; i++) {
            ob[i] = (u32x4){0u, 0u, 0u, 0u};
            if (i < 7 || i * 16384 + wave * 1024 < olen) ob[i] = *LDSP(u32x4, (i < 4 ? a0 : i < 8 ? a1 : a2) + (i & 3) * 16384);
        }
    };
    auto put_out = [&](int i) __attribute__((always_inline)) {
        if (i < 7 || i * 16384 + wave * 1024 < olen) __builtin_amdgcn_raw_buffer_store_b128(ob[i], dst, opaque(threadIdx.x) * 16 + i * 16384, 0, ST_AUX);
    };

    if (FULL) {
#pragma unroll
        for (int i = 0; i < 6; i++) {
            proc_quad<FULL, 0>(r[i], c, 0, 0, w + 4 * i, h + 4 * i, nanacc);
            SB();
            if (i == 0) __builtin_amdgcn_s_setprio(2);
            if (i == 1) __builtin_amdgcn_s_setprio(1);
            if (i == 2) __builtin_amdgcn_s_setprio(0);
            if (i < 4) load_quad<FULL>(r[i + 2], src, opaque(tid), i + 2);
            if (i == 4) load_single();
            SB();
        }
        hist_single(tid < nsingle);
        SB();
        asm volatile("" :: "s"(tq[0]), "s"(tq[1]), "s"(tq[2]), "s"(tq[3]), "s"(tq[4]), "s"(tq[5]), "s"(tq[6]), "s"(tq[7]));
        asm volatile("" :: "s"(*(cptr)A.w_down), "s"(*(cptr)A.w_up));
        asm volatile("s_waitcnt vmcnt(0)" ::: "memory");
        u32 asked = 0;
        if (blockIdx.x == 0 && tid >= 768) asked = ld_agent(ask + (tid - 768));
#define LQ(i) { SB(); load_quad<FULL>(r[i], src, opaque(tid), 6 + (i)); SB(); }
        LQ(0);
        lds_barrier();
        const u32 lenA = scan();
        if (blockIdx.x == 0 && tid >= 768) st_agent(echo + (tid - 768), asked);
        lds_barrier();
        get_table();
#pragma unroll
        for (int i = 0; i < 6; i++) {
            if (i % 2 == 0) LQ(1 + i / 2);
            scatter_quad<FULL>(w + 4 * i, h + 4 * i);
        }
        scatter_single();
        lds_barrier();
        LQ(4); LQ(5);
        hist_quads(IntC<H2>{}, 0);
        hoff = H2;
        SB();
        const u32x2 tabA = tabv;
        read_out(chunk, lenA);
        put_out(0);
        NEW_PAGE(1);
        {
        lds_barrier();
        u32 heard = 0;
        if (wave == 0) heard = ld_agent(echo + blockIdx.x);
        if (tid == 0) { *LDSP(u32, K1_GO) = 0; *LDSP(u32x4, K2_MISC + 64) = (u32x4){1u, 0u, 0u, 0u}; }
        const u32 lenB = scan();
        {
            const u32 mxw = wave_incl_max(nanacc);
            if (lane == 63) *LDSP(u32, K1_WMAX + wave * 4) = mxw;
        }
        lds_barrier();
        get_table();
        __builtin_amdgcn_raw_buffer_store_b128((u32x4){tabA.x, tabA.y, tabv.x, tabv.y},
                                               make_rsrc(A.offs + (size_t)chunk * OFF_STRIDE, OFF_STRIDE * 4), opaque(tid) * 16, 0, ST_AUX);
#define PO(i) { SB(); put_out(i); SB(); }
        PO(2); scatter_quad<FULL, H2>(w + 0, h + 0);
        PO(3); scatter_quad<FULL, H2>(w + 4, h + 4);
        PO(4); PO(5); scatter_quad<FULL, H2>(w + 8, h + 8);
        PO(6); PO(7); scatter_quad<FULL, H2>(w + 12, h + 12);
        PO(8); PO(1); scatter_quad<FULL, H2>(w + 16, h + 16);
        scatter_quad<FULL, H2>(w + 20, h + 20);
        NEW_PAGE(2);
        auto reecho = [&]() __attribute__((always_inline)) {
            const u32x4 q = __builtin_amdgcn_raw_buffer_load_b128(rs_sync, WS_ASK + lane * 16, 0, AUX_SC1);
            const u32x4 e = __builtin_amdgcn_raw_buffer_load_b128(rs_sync, WS_ECHO + lane * 16, 0, AUX_SC1);
            if (q.x != e.x || q.y != e.y || q.z != e.z || q.w != e.w)
                __builtin_amdgcn_raw_buffer_store_b128(q, rs_sync, WS_ECHO + lane * 16, 0, AUX_SC1);
        };
        asm volatile("s_waitcnt vmcnt(6)" : "+v"(heard) :: "memory");
        bool heard_ok = true;
        if (wave == 0) {
            u32 spin = 0;
#pragma unroll 1
            while (__builtin_amdgcn_readfirstlane(heard) != __builtin_amdgcn_readfirstlane(chal) && ++spin < (1u << 22)) {
                if (blockIdx.x == 0) reecho();
                __builtin_amdgcn_s_sleep(2);
                CBAR();
                heard = opaque(ld_agent(echo + blockIdx.x));
            }
            heard_ok = spin < (1u << 22);
        }
        {
            const u32 inc = heard_ok ? 1u : 0x101u;
            u32 before = 0;
            if (lane == 0) before = __hip_atomic_fetch_add(LDSP(u32, K1_GO), inc, __ATOMIC_RELAXED, __HIP_MEMORY_SCOPE_WORKGROUP);
            if (__builtin_amdgcn_readfirstlane(before) + inc == 16) {
                const u32 mine = __builtin_amdgcn_readlane(wave_incl_max(*LDSP(u32, K1_WMAX + (lane & 15) * 4)), 63) | 0x80000000u;
                if (lane < NREP) st_agent(flags + lane * NWG + blockIdx.x, mine);
            }
        }
        lds_barrier();
        const u16 wf = load_wfrag(A, opaque(tid));
        read_out(chunk + NWG, lenB);
        put_out(0);
        lds_barrier();
#pragma unroll
        for (int i = 1; i < 9; i++) put_out(i);
        SB();
        prep_lds(wf, opaque(tid), true);
        if (wave == 0) {
            u32 r = 0xc0000000u;
#pragma unroll 1
            for (u32 spin = 0; heard_ok && spin < (1u << 22); spin++) {
                CBAR();
                const u32x4 f = __builtin_amdgcn_raw_buffer_load_b128(rs_sync, WS_FLAG + (blockIdx.x % NREP) * 1024 + lane * 16, 0, AUX_SC1);
                if (__all((int)(f.x & f.y & f.z & f.w) < 0)) {
                    r = __builtin_amdgcn_readlane(wave_incl_max(umax(umax(f.x, f.y), umax(f.z, f.w))), 63) ^ 0xc0000000u;
                    break;
                }
                if (blockIdx.x == 0) reecho();
                __builtin_amdgcn_s_sleep(8);
            }
            CBAR();
            if (lane == 0) *LDSP(u32, K2_MISC + 72) = r;
        }
        u32 res;
        for (;;) {
            CBAR();
            res = __builtin_amdgcn_readfirstlane(*LDSP(u32, K2_MISC + 72));
            if (res) break;
            __builtin_amdgcn_s_sleep(4);
        }
        CBAR();
        return res & ~0x40000000u;
        }
    } else {
        while (chunk < (nchunks + 511) / 512 * 512) {
            hist_quads(IntC<0>{}, nq);
            hist_single(nq * 4 + tid < n);
            lds_barrier();
            const u32 len = scan();
            lds_barrier();
            get_table();
            *(u32x2*)(A.offs + tab_index(chunk, 0, 0) + opaque(tid) * 4) = tabv;
#pragma unroll
            for (int i = 0; i < 6; i++) scatter_quad<FULL>(w + 4 * i, h + 4 * i);
            scatter_single();
            SB();
            setup(chunk + NWG);
            const u32 t = opaque(tid);
#pragma unroll
            for (int i = 0; i < 6; i++) load_quad<FULL>(r[i], src, t, i);
            load_single();
            SB();
            lds_barrier();
            zero_hist();
            read_out(chunk, len);
#pragma unroll
            for (int i = 0; i < 9; i++) put_out(i);
            lds_barrier();
            chunk += NWG;
        }
    }
    u32 mx = wave_incl_max(nanacc);
    if (lane == 63) *LDSP(u32, K1_WMAX + wave * 4) = mx;
    lds_barrier();
    if (tid < 64) {
        u32 v = *LDSP(u32, K1_WMAX + (lane & 15) * 4);
        v = wave_incl_max(v);
        if (tid == 63) flags[blockIdx.x] = v;
    }
    return 0;
}

DEVI void lds_addf(u32 addr, float v) { asm volatile("ds_add_f32 %0, %1" :: "v"(addr), "v"(v) : "memory"); }

DEVI void k2_slow(const Args& A, u32 bucket, u32 g) {
    const u32 tid = threadIdx.x;
    for (u32 i = tid; i < RO; i += 1024) *LDSP(u32x4, K2_ACCA + i * 16) = (u32x4){0u, 0u, 0u, 0u};
    lds_barrier();
    for (u32 kc = tid >> 4; kc < A.nchunks * 8; kc += 64) {
        u32 k = kc >> 3, st = kc & 7;
        const u32 ent = ld_agent(A.offs + tab_index(k, bucket, st));
        u32 o0 = ent & 0xffffu, o1 = o0 + (ent >> 16);
        const u32* rp = (const u32*)(A.ws + WS_REC + (size_t)k * CHB);
        for (u32 i = o0 + (tid & 15); i < o1; i += 16) {
            const u32 w = ld_agent(rp + (i >> 2) * 5 + (i & 3) + ((i >> 1) & 1));
            float cf; u32 row1, il;
            unpack_rec(w, (ld_agent(rp + (i >> 2) * 5 + 2) >> ((i & 3) * 8)) & 0xffu, cf, row1, il);
            const u32 oa = K2_ACC0 + row1 * 16;
            u32x2 v = A.m2[((g * NST + st) * 2 + ((w >> 30) & 1)) * RI + il];
            if (__builtin_fabsf(cf) > 1.0f) cf = __uint_as_float(0x7fc00000u);
            lds_addf(oa, cf * bf2f(v.x));
            lds_addf(oa + 4, cf * __uint_as_float(v.x & 0xffff0000u));
            lds_addf(oa + 8, cf * bf2f(v.y));
            lds_addf(oa + 12, cf * __uint_as_float(v.y & 0xffff0000u));
        }
    }
    lds_barrier();
}

#define K2_COOP 1
#define K2_EXMIN 0
#define K2_ASM_TEXT \
    "s_branch 11f\n\t" \
    ".p2align 12\n\t" \
    "kw%c14_0:\n\t" \
    "s_setpc_b64 vcc\n\t" \
    "11:\n\t" \
    "v_and_b32 v83, 63, %3\n\t" \
    "v_lshlrev_b32 v104, 4, %3\n\t" \
    "v_and_b32 v100, 60, v83\n\t" \
    "v_lshl_add_u32 v100, v100, 2, %8\n\t" \
    "v_and_b32 v101, 3, v83\n\t" \
    "v_lshlrev_b32 v102, 14, v100\n\t" \
    "v_lshl_add_u32 v102, v101, 4, v102\n\t" \
    "global_load_dwordx4 v[68:71], v102, %13 sc1\n\t" \
    "v_lshl_add_u32 v100, v101, 8, v100\n\t" \
    "v_cmp_eq_u32 vcc, 0, v101\n\t" \
    "v_mov_b32 %0, 0\n\t" \
    "s_nop 0\n\t" \
    "v_cndmask_b32 v72, 0, -1, vcc\n\t" \
    "v_cmp_eq_u32 vcc, 1, v101\n\t" \
    "s_nop 1\n\t" \
    "v_cndmask_b32 v73, 0, -1, vcc\n\t" \
    "v_cmp_gt_u32 vcc, 2, v101\n\t" \
    "v_mul_u32_u24 v76, 137088, v100\n\t" \
    "v_lshlrev_b32 v105, 3, %3\n\t" \
    "s_add_u32 s94, %7, 57152\n\t" \
    "s_movk_i32 s87, 1000\n\t" \
    "v_cmp_gt_u32 s[80:81], s87, %3\n\t" \
    "v_bfrev_b32 v102, 3\n\t" \
    "v_cndmask_b32 v76, v102, v76, vcc\n\t" \
    "v_cmp_eq_u32 vcc, 63, v83\n\t" \
    "v_mul_u32_u24 v103, 10, v83\n\t" \
    "v_mov_b32 v106, 1\n\t" \
    "v_mov_b32 v79, 252\n\t" \
    "v_cndmask_b32 v77, 0, -1, vcc\n\t" \
    "s_mul_i32 s85, %8, 320\n\t" \
    "s_add_u32 s85, s85, 57152\n\t" \
    "s_sub_u32 s87, s85, 160\n\t" \
    "v_lshl_add_u32 v78, v83, 2, s87\n\t" \
    "s_mov_b32 s95, 62272\n\t" \
    "v_and_b32 v112, 1, v83\n\t" \
    "v_lshl_add_u32 v112, v112, 3, s95\n\t" \
    "v_mov_b32 v113, 0x77800000\n\t" \
    "v_mov_b32 v114, 0x020c030c\n\t" \
    "s_mov_b32 s88, 0x050c040c\n\t" \
    "s_mov_b32 s89, 0x0c040c00\n\t" \
    "s_mov_b32 s90, 0x00ff00ff\n\t" \
    "s_mov_b32 s82, 0x55555555\n\t" \
    "s_mov_b32 s83, 0x55555555\n\t" \
    "s_mov_b32 %1, 0\n\t" \
    "s_mov_b32 s91, 0\n\t" \
    "s_mov_b32 s78, %6\n\t" \
    "s_mov_b32 s79, %6\n\t" \
    "s_sub_u32 s84, 0x4000000, %6\n\t" \
    "s_waitcnt vmcnt(1)\n\t" \
    "s_cmpk_gt_u32 %12, 255\n\t" \
    "s_cbranch_scc1 3f\n\t" \
    "v_mov_b32 v100, 163820\n\t" \
    "s_mov_b64 exec, 1\n\t" \
    "ds_add_rtn_u32 v101, v100, v106\n\t" \
    "s_mov_b64 exec, -1\n\t" \
    "s_and_b32 s86, %12, 7\n\t" \
    "s_lshl_b32 s87, s86, 8\n\t" \
    "v_mov_b32 v100, s87\n\t" \
    "s_waitcnt lgkmcnt(0)\n\t" \
    "v_readfirstlane_b32 s87, v101\n\t" \
    "s_cmp_lg_u32 s87, 15\n\t" \
    "s_cbranch_scc1 3f\n\t" \
    "s_mov_b64 exec, 1\n\t" \
    "global_atomic_add v101, v100, v106, %11 sc0\n\t" \
    "s_mov_b64 exec, -1\n\t" \
    "s_lshl_b32 s86, s86, 2\n\t" \
    "v_lshl_add_u32 v100, v83, 8, s86\n\t" \
    "s_waitcnt vmcnt(0)\n\t" \
    "v_readfirstlane_b32 s87, v101\n\t" \
    "s_cmpk_lg_u32 s87, 31\n\t" \
    "s_cbranch_scc1 3f\n\t" \
    "s_mov_b64 exec, 0xffff\n\t" \
    "global_store_dword v100, v106, %11 offset:128 sc1\n\t" \
    "s_mov_b64 exec, -1\n\t" \
    "3:\n\t" \
    "s_add_u32 s87, %7, 0\n\t" \
    "buffer_load_dwordx4 v[60:63], v104, %4, s87 offen\n\t" \
    "s_add_u32 s87, %7, 16384\n\t" \
    "buffer_load_dwordx4 v[64:67], v104, %4, s87 offen\n\t" \
    "s_add_u32 s87, %7, 32768\n\t" \
    "buffer_load_dwordx4 v[84:87], v104, %4, s87 offen\n\t" \
    "s_add_u32 s87, %7, 49152\n\t" \
    "buffer_load_dwordx2 v[88:89], v105, %4, s87 offen\n\t" \
    "s_waitcnt vmcnt(4)\n\t" \
    "v_and_b32_dpp v97, v68, v72 quad_perm:[0,0,0,0] row_mask:0xf bank_mask:0xf\n\t" \
    "v_and_b32_dpp v98, v70, v73 quad_perm:[0,0,0,0] row_mask:0xf bank_mask:0xf\n\t" \
    "v_or_b32 v97, v97, v98\n\t" \
    "v_add_u32_sdwa %0, v97, %0 dst_sel:DWORD dst_unused:UNUSED_PAD src0_sel:WORD_1 src1_sel:DWORD\n\t" \
    "v_add_u32 v92, 0x10000, v97\n\t" \
    "v_lshrrev_b32 v92, 17, v92\n\t" \
    "v_cmp_ne_u32 vcc, v77, v92\n\t" \
    "v_bfe_u32 v96, v97, 1, 15\n\t" \
    "v_add_u32_dpp v93, v92, v92 row_shr:1 row_mask:0xf bank_mask:0xf bound_ctrl:1\n\t" \
    "v_mbcnt_lo_u32_b32 v95, vcc_lo, 0\n\t" \
    "v_mbcnt_hi_u32_b32 v95, vcc_hi, v95\n\t" \
    "v_add_u32_dpp v93, v93, v93 row_shr:2 row_mask:0xf bank_mask:0xf bound_ctrl:1\n\t" \
    "v_lshlrev_b32 v95, 2, v95\n\t" \
    "v_cndmask_b32 v95, v79, v95, vcc\n\t" \
    "v_add_u32_dpp v93, v93, v93 row_shr:4 row_mask:0xf bank_mask:0xf bound_ctrl:1\n\t" \
    "s_nop 1\n\t" \
    "v_add_u32_dpp v93, v93, v93 row_shr:8 row_mask:0xf bank_mask:0xf bound_ctrl:1\n\t" \
    "s_nop 1\n\t" \
    "v_add_u32_dpp v93, v93, v93 row_bcast:15 row_mask:0xa bank_mask:0xf\n\t" \
    "s_nop 1\n\t" \
    "v_add_u32_dpp v93, v93, v93 row_bcast:31 row_mask:0xc bank_mask:0xf\n\t" \
    "v_sub_u32 v94, v93, v92\n\t" \
    "v_readlane_b32 s93, v93, 63\n\t" \
    "v_sub_u32 v96, v96, v94\n\t" \
    "v_add_u32 v94, -1, v94\n\t" \
    "v_mad_i32_i24 v96, v96, 10, v76\n\t" \
    "v_lshrrev_b32 v92, 5, v94\n\t" \
    "v_lshl_add_u32 v92, v92, 2, s85\n\t" \
    "v_lshlrev_b32 v93, v94, v106\n\t" \
    "ds_permute_b32 v81, v95, v96\n\t" \
    "s_mov_b64 exec, vcc\n\t" \
    "v_cmpx_gt_u32 vcc, 320, v94\n\t" \
    "ds_or_b32 v92, v93 offset:0\n\t" \
    "s_mov_b64 exec, -1\n\t" \
    "s_max_u32 %1, %1, s93\n\t" \
    "s_cmpk_gt_u32 s93, 256\n\t" \
    "s_cselect_b32 s93, 1, 0\n\t" \
    "s_or_b32 s91, s91, s93\n\t" \
    "v_and_b32_dpp v97, v69, v72 quad_perm:[0,0,0,0] row_mask:0xf bank_mask:0xf\n\t" \
    "v_and_b32_dpp v98, v71, v73 quad_perm:[0,0,0,0] row_mask:0xf bank_mask:0xf\n\t" \
    "v_or_b32 v97, v97, v98\n\t" \
    "v_add_u32_sdwa %0, v97, %0 dst_sel:DWORD dst_unused:UNUSED_PAD src0_sel:WORD_1 src1_sel:DWORD\n\t" \
    "v_add_u32 v92, 0x10000, v97\n\t" \
    "v_lshrrev_b32 v92, 17, v92\n\t" \
    "v_cmp_ne_u32 vcc, v77, v92\n\t" \
    "v_bfe_u32 v96, v97, 1, 15\n\t" \
    "v_add_u32_dpp v93, v92, v92 row_shr:1 row_mask:0xf bank_mask:0xf bound_ctrl:1\n\t" \
    "v_mbcnt_lo_u32_b32 v95, vcc_lo, 0\n\t" \
    "v_mbcnt_hi_u32_b32 v95, vcc_hi, v95\n\t" \
    "v_add_u32_dpp v93, v93, v93 row_shr:2 row_mask:0xf bank_mask:0xf bound_ctrl:1\n\t" \
    "v_lshlrev_b32 v95, 2, v95\n\t" \
    "v_cndmask_b32 v95, v79, v95, vcc\n\t" \
    "v_add_u32_dpp v93, v93, v93 row_shr:4 row_mask:0xf bank_mask:0xf bound_ctrl:1\n\t" \
    "s_nop 1\n\t" \
    "v_add_u32_dpp v93, v93, v93 row_shr:8 row_mask:0xf bank_mask:0xf bound_ctrl:1\n\t" \
    "s_nop 1\n\t" \
    "v_add_u32_dpp v93, v93, v93 row_bcast:15 row_mask:0xa bank_mask:0xf\n\t" \
    "s_nop 1\n\t" \
    "v_add_u32_dpp v93, v93, v93 row_bcast:31 row_mask:0xc bank_mask:0xf\n\t" \
    "v_sub_u32 v94, v93, v92\n\t" \
    "v_readlane_b32 s93, v93, 63\n\t" \
    "v_sub_u32 v96, v96, v94\n\t" \
    "v_add_u32 v94, -1, v94\n\t" \
    "v_mad_i32_i24 v96, v96, 10, v76\n\t" \
    "v_lshrrev_b32 v92, 5, v94\n\t" \
    "v_lshl_add_u32 v92, v92, 2, s85\n\t" \
    "v_lshlrev_b32 v93, v94, v106\n\t" \
    "ds_permute_b32 v82, v95, v96\n\t" \
    "s_mov_b64 exec, vcc\n\t" \
    "v_cmpx_gt_u32 vcc, 320, v94\n\t" \
    "ds_or_b32 v92, v93 offset:40\n\t" \
    "s_mov_b64 exec, -1\n\t" \
    "s_max_u32 %1, %1, s93\n\t" \
    "s_cmpk_gt_u32 s93, 256\n\t" \
    "s_cselect_b32 s93, 2, 0\n\t" \
    "s_or_b32 s91, s91, s93\n\t" \
    "v_and_b32_dpp v97, v68, v72 quad_perm:[1,1,1,1] row_mask:0xf bank_mask:0xf\n\t" \
    "v_and_b32_dpp v98, v70, v73 quad_perm:[1,1,1,1] row_mask:0xf bank_mask:0xf\n\t" \
    "v_or_b32 v97, v97, v98\n\t" \
    "v_add_u32_sdwa %0, v97, %0 dst_sel:DWORD dst_unused:UNUSED_PAD src0_sel:WORD_1 src1_sel:DWORD\n\t" \
    "v_add_u32 v92, 0x10000, v97\n\t" \
    "v_lshrrev_b32 v92, 17, v92\n\t" \
    "v_cmp_ne_u32 vcc, v77, v92\n\t" \
    "v_bfe_u32 v96, v97, 1, 15\n\t" \
    "v_add_u32_dpp v93, v92, v92 row_shr:1 row_mask:0xf bank_mask:0xf bound_ctrl:1\n\t" \
    "v_mbcnt_lo_u32_b32 v95, vcc_lo, 0\n\t" \
    "v_mbcnt_hi_u32_b32 v95, vcc_hi, v95\n\t" \
    "v_add_u32_dpp v93, v93, v93 row_shr:2 row_mask:0xf bank_mask:0xf bound_ctrl:1\n\t" \
    "v_lshlrev_b32 v95, 2, v95\n\t" \
    "v_cndmask_b32 v95, v79, v95, vcc\n\t" \
    "v_add_u32_dpp v93, v93, v93 row_shr:4 row_mask:0xf bank_mask:0xf bound_ctrl:1\n\t" \
    "s_nop 1\n\t" \
    "v_add_u32_dpp v93, v93, v93 row_shr:8 row_mask:0xf bank_mask:0xf bound_ctrl:1\n\t" \
    "s_nop 1\n\t" \
    "v_add_u32_dpp v93, v93, v93 row_bcast:15 row_mask:0xa bank_mask:0xf\n\t" \
    "s_nop 1\n\t" \
    "v_add_u32_dpp v93, v93, v93 row_bcast:31 row_mask:0xc bank_mask:0xf\n\t" \
    "v_sub_u32 v94, v93, v92\n\t" \
    "v_readlane_b32 s93, v93, 63\n\t" \
    "v_sub_u32 v96, v96, v94\n\t" \
    "v_add_u32 v94, -1, v94\n\t" \
    "v_mad_i32_i24 v96, v96, 10, v76\n\t" \
    "v_lshrrev_b32 v92, 5, v94\n\t" \
    "v_lshl_add_u32 v92, v92, 2, s85\n\t" \
    "v_lshlrev_b32 v93, v94, v106\n\t" \
    "ds_permute_b32 v107, v95, v96\n\t" \
    "s_mov_b64 exec, vcc\n\t" \
    "v_cmpx_gt_u32 vcc, 320, v94\n\t" \
    "ds_or_b32 v92, v93 offset:80\n\t" \
    "s_mov_b64 exec, -1\n\t" \
    "s_max_u32 %1, %1, s93\n\t" \
    "s_cmpk_gt_u32 s93, 256\n\t" \
    "s_cselect_b32 s93, 4, 0\n\t" \
    "s_or_b32 s91, s91, s93\n\t" \
    "v_and_b32_dpp v97, v69, v72 quad_perm:[1,1,1,1] row_mask:0xf bank_mask:0xf\n\t" \
    "v_and_b32_dpp v98, v71, v73 quad_perm:[1,1,1,1] row_mask:0xf bank_mask:0xf\n\t" \
    "v_or_b32 v97, v97, v98\n\t" \
    "v_add_u32_sdwa %0, v97, %0 dst_sel:DWORD dst_unused:UNUSED_PAD src0_sel:WORD_1 src1_sel:DWORD\n\t" \
    "v_add_u32 v92, 0x10000, v97\n\t" \
    "v_lshrrev_b32 v92, 17, v92\n\t" \
    "v_cmp_ne_u32 vcc, v77, v92\n\t" \
    "v_bfe_u32 v96, v97, 1, 15\n\t" \
    "v_add_u32_dpp v93, v92, v92 row_shr:1 row_mask:0xf bank_mask:0xf bound_ctrl:1\n\t" \
    "v_mbcnt_lo_u32_b32 v95, vcc_lo, 0\n\t" \
    "v_mbcnt_hi_u32_b32 v95, vcc_hi, v95\n\t" \
    "v_add_u32_dpp v93, v93, v93 row_shr:2 row_mask:0xf bank_mask:0xf bound_ctrl:1\n\t" \
    "v_lshlrev_b32 v95, 2, v95\n\t" \
    "v_cndmask_b32 v95, v79, v95, vcc\n\t" \
    "v_add_u32_dpp v93, v93, v93 row_shr:4 row_mask:0xf bank_mask:0xf bound_ctrl:1\n\t" \
    "s_nop 1\n\t" \
    "v_add_u32_dpp v93, v93, v93 row_shr:8 row_mask:0xf bank_mask:0xf bound_ctrl:1\n\t" \
    "s_nop 1\n\t" \
    "v_add_u32_dpp v93, v93, v93 row_bcast:15 row_mask:0xa bank_mask:0xf\n\t" \
    "s_nop 1\n\t" \
    "v_add_u32_dpp v93, v93, v93 row_bcast:31 row_mask:0xc bank_mask:0xf\n\t" \
    "v_sub_u32 v94, v93, v92\n\t" \
    "v_readlane_b32 s93, v93, 63\n\t" \
    "v_sub_u32 v96, v96, v94\n\t" \
    "v_add_u32 v94, -1, v94\n\t" \
    "v_mad_i32_i24 v96, v96, 10, v76\n\t" \
    "v_lshrrev_b32 v92, 5, v94\n\t" \
    "v_lshl_add_u32 v92, v92, 2, s85\n\t" \
    "v_lshlrev_b32 v93, v94, v106\n\t" \
    "ds_permute_b32 v108, v95, v96\n\t" \
    "s_mov_b64 exec, vcc\n\t" \
    "v_cmpx_gt_u32 vcc, 320, v94\n\t" \
    "ds_or_b32 v92, v93 offset:120\n\t" \
    "s_mov_b64 exec, -1\n\t" \
    "s_max_u32 %1, %1, s93\n\t" \
    "s_cmpk_gt_u32 s93, 256\n\t" \
    "s_cselect_b32 s93, 8, 0\n\t" \
    "s_or_b32 s91, s91, s93\n\t" \
    "v_and_b32_dpp v97, v68, v72 quad_perm:[2,2,2,2] row_mask:0xf bank_mask:0xf\n\t" \
    "v_and_b32_dpp v98, v70, v73 quad_perm:[2,2,2,2] row_mask:0xf bank_mask:0xf\n\t" \
    "v_or_b32 v97, v97, v98\n\t" \
    "v_add_u32_sdwa %0, v97, %0 dst_sel:DWORD dst_unused:UNUSED_PAD src0_sel:WORD_1 src1_sel:DWORD\n\t" \
    "v_add_u32 v92, 0x10000, v97\n\t" \
    "v_lshrrev_b32 v92, 17, v92\n\t" \
    "v_cmp_ne_u32 vcc, v77, v92\n\t" \
    "v_bfe_u32 v96, v97, 1, 15\n\t" \
    "v_add_u32_dpp v93, v92, v92 row_shr:1 row_mask:0xf bank_mask:0xf bound_ctrl:1\n\t" \
    "v_mbcnt_lo_u32_b32 v95, vcc_lo, 0\n\t" \
    "v_mbcnt_hi_u32_b32 v95, vcc_hi, v95\n\t" \
    "v_add_u32_dpp v93, v93, v93 row_shr:2 row_mask:0xf bank_mask:0xf bound_ctrl:1\n\t" \
    "v_lshlrev_b32 v95, 2, v95\n\t" \
    "v_cndmask_b32 v95, v79, v95, vcc\n\t" \
    "v_add_u32_dpp v93, v93, v93 row_shr:4 row_mask:0xf bank_mask:0xf bound_ctrl:1\n\t" \
    "s_nop 1\n\t" \
    "v_add_u32_dpp v93, v93, v93 row_shr:8 row_mask:0xf bank_mask:0xf bound_ctrl:1\n\t" \
    "s_nop 1\n\t" \
    "v_add_u32_dpp v93, v93, v93 row_bcast:15 row_mask:0xa bank_mask:0xf\n\t" \
    "s_nop 1\n\t" \
    "v_add_u32_dpp v93, v93, v93 row_bcast:31 row_mask:0xc bank_mask:0xf\n\t" \
    "v_sub_u32 v94, v93, v92\n\t" \
    "v_readlane_b32 s93, v93, 63\n\t" \
    "v_sub_u32 v96, v96, v94\n\t" \
    "v_add_u32 v94, -1, v94\n\t" \
    "v_mad_i32_i24 v96, v96, 10, v76\n\t" \
    "v_lshrrev_b32 v92, 5, v94\n\t" \
    "v_lshl_add_u32 v92, v92, 2, s85\n\t" \
    "v_lshlrev_b32 v93, v94, v106\n\t" \
    "ds_permute_b32 v109, v95, v96\n\t" \
    "s_mov_b64 exec, vcc\n\t" \
    "v_cmpx_gt_u32 vcc, 320, v94\n\t" \
    "ds_or_b32 v92, v93 offset:160\n\t" \
    "s_mov_b64 exec, -1\n\t" \
    "s_max_u32 %1, %1, s93\n\t" \
    "s_cmpk_gt_u32 s93, 256\n\t" \
    "s_cselect_b32 s93, 16, 0\n\t" \
    "s_or_b32 s91, s91, s93\n\t" \
    "v_and_b32_dpp v97, v69, v72 quad_perm:[2,2,2,2] row_mask:0xf bank_mask:0xf\n\t" \
    "v_and_b32_dpp v98, v71, v73 quad_perm:[2,2,2,2] row_mask:0xf bank_mask:0xf\n\t" \
    "v_or_b32 v97, v97, v98\n\t" \
    "v_add_u32_sdwa %0, v97, %0 dst_sel:DWORD dst_unused:UNUSED_PAD src0_sel:WORD_1 src1_sel:DWORD\n\t" \
    "v_add_u32 v92, 0x10000, v97\n\t" \
    "v_lshrrev_b32 v92, 17, v92\n\t" \
    "v_cmp_ne_u32 vcc, v77, v92\n\t" \
    "v_bfe_u32 v96, v97, 1, 15\n\t" \
    "v_add_u32_dpp v93, v92, v92 row_shr:1 row_mask:0xf bank_mask:0xf bound_ctrl:1\n\t" \
    "v_mbcnt_lo_u32_b32 v95, vcc_lo, 0\n\t" \
    "v_mbcnt_hi_u32_b32 v95, vcc_hi, v95\n\t" \
    "v_add_u32_dpp v93, v93, v93 row_shr:2 row_mask:0xf bank_mask:0xf bound_ctrl:1\n\t" \
    "v_lshlrev_b32 v95, 2, v95\n\t" \
    "v_cndmask_b32 v95, v79, v95, vcc\n\t" \
    "v_add_u32_dpp v93, v93, v93 row_shr:4 row_mask:0xf bank_mask:0xf bound_ctrl:1\n\t" \
    "s_nop 1\n\t" \
    "v_add_u32_dpp v93, v93, v93 row_shr:8 row_mask:0xf bank_mask:0xf bound_ctrl:1\n\t" \
    "s_nop 1\n\t" \
    "v_add_u32_dpp v93, v93, v93 row_bcast:15 row_mask:0xa bank_mask:0xf\n\t" \
    "s_nop 1\n\t" \
    "v_add_u32_dpp v93, v93, v93 row_bcast:31 row_mask:0xc bank_mask:0xf\n\t" \
    "v_sub_u32 v94, v93, v92\n\t" \
    "v_readlane_b32 s93, v93, 63\n\t" \
    "v_sub_u32 v96, v96, v94\n\t" \
    "v_add_u32 v94, -1, v94\n\t" \
    "v_mad_i32_i24 v96, v96, 10, v76\n\t" \
    "v_lshrrev_b32 v92, 5, v94\n\t" \
    "v_lshl_add_u32 v92, v92, 2, s85\n\t" \
    "v_lshlrev_b32 v93, v94, v106\n\t" \
    "ds_permute_b32 v110, v95, v96\n\t" \
    "s_mov_b64 exec, vcc\n\t" \
    "v_cmpx_gt_u32 vcc, 320, v94\n\t" \
    "ds_or_b32 v92, v93 offset:200\n\t" \
    "s_mov_b64 exec, -1\n\t" \
    "s_max_u32 %1, %1, s93\n\t" \
    "s_cmpk_gt_u32 s93, 256\n\t" \
    "s_cselect_b32 s93, 32, 0\n\t" \
    "s_or_b32 s91, s91, s93\n\t" \
    "v_and_b32_dpp v97, v68, v72 quad_perm:[3,3,3,3] row_mask:0xf bank_mask:0xf\n\t" \
    "v_and_b32_dpp v98, v70, v73 quad_perm:[3,3,3,3] row_mask:0xf bank_mask:0xf\n\t" \
    "v_or_b32 v97, v97, v98\n\t" \
    "v_add_u32_sdwa %0, v97, %0 dst_sel:DWORD dst_unused:UNUSED_PAD src0_sel:WORD_1 src1_sel:DWORD\n\t" \
    "v_add_u32 v92, 0x10000, v97\n\t" \
    "v_lshrrev_b32 v92, 17, v92\n\t" \
    "v_cmp_ne_u32 vcc, v77, v92\n\t" \
    "v_bfe_u32 v96, v97, 1, 15\n\t" \
    "v_add_u32_dpp v93, v92, v92 row_shr:1 row_mask:0xf bank_mask:0xf bound_ctrl:1\n\t" \
    "v_mbcnt_lo_u32_b32 v95, vcc_lo, 0\n\t" \
    "v_mbcnt_hi_u32_b32 v95, vcc_hi, v95\n\t" \
    "v_add_u32_dpp v93, v93, v93 row_shr:2 row_mask:0xf bank_mask:0xf bound_ctrl:1\n\t" \
    "v_lshlrev_b32 v95, 2, v95\n\t" \
    "v_cndmask_b32 v95, v79, v95, vcc\n\t" \
    "v_add_u32_dpp v93, v93, v93 row_shr:4 row_mask:0xf bank_mask:0xf bound_ctrl:1\n\t" \
    "s_nop 1\n\t" \
    "v_add_u32_dpp v93, v93, v93 row_shr:8 row_mask:0xf bank_mask:0xf bound_ctrl:1\n\t" \
    "s_nop 1\n\t" \
    "v_add_u32_dpp v93, v93, v93 row_bcast:15 row_mask:0xa bank_mask:0xf\n\t" \
    "s_nop 1\n\t" \
    "v_add_u32_dpp v93, v93, v93 row_bcast:31 row_mask:0xc bank_mask:0xf\n\t" \
    "v_sub_u32 v94, v93, v92\n\t" \
    "v_readlane_b32 s93, v93, 63\n\t" \
    "v_sub_u32 v96, v96, v94\n\t" \
    "v_add_u32 v94, -1, v94\n\t" \
    "v_mad_i32_i24 v96, v96, 10, v76\n\t" \
    "v_lshrrev_b32 v92, 5, v94\n\t" \
    "v_lshl_add_u32 v92, v92, 2, s85\n\t" \
    "v_lshlrev_b32 v93, v94, v106\n\t" \
    "ds_permute_b32 v111, v95, v96\n\t" \
    "s_mov_b64 exec, vcc\n\t" \
    "v_cmpx_gt_u32 vcc, 320, v94\n\t" \
    "ds_or_b32 v92, v93 offset:240\n\t" \
    "s_mov_b64 exec, -1\n\t" \
    "s_max_u32 %1, %1, s93\n\t" \
    "s_cmpk_gt_u32 s93, 256\n\t" \
    "s_cselect_b32 s93, 64, 0\n\t" \
    "s_or_b32 s91, s91, s93\n\t" \
    "s_mov_b32 exec_lo, 0\n\t" \
    "s_mov_b32 exec_hi, 0x3ff00\n\t" \
    "ds_read_b32 v81, v78 offset:0\n\t" \
    "ds_read_b32 v82, v78 offset:40\n\t" \
    "ds_read_b32 v107, v78 offset:80\n\t" \
    "ds_read_b32 v108, v78 offset:120\n\t" \
    "ds_read_b32 v109, v78 offset:160\n\t" \
    "ds_read_b32 v110, v78 offset:200\n\t" \
    "ds_read_b32 v111, v78 offset:240\n\t" \
    "s_mov_b64 exec, -1\n\t" \
    "s_waitcnt vmcnt(0)\n\t" \
    "ds_write_b128 v104, v[60:63] offset:0\n\t" \
    "ds_write_b128 v104, v[64:67] offset:16384\n\t" \
    "ds_write_b128 v104, v[84:87] offset:32768\n\t" \
    "s_mov_b64 exec, s[80:81]\n\t" \
    "ds_write_b64 v105, v[88:89] offset:49152\n\t" \
    "s_mov_b64 exec, -1\n\t" \
    "s_waitcnt lgkmcnt(4)\n\t" \
    "v_readlane_b32 s68, v81, 40\n\t" \
    "v_readlane_b32 s69, v81, 41\n\t" \
    "v_readlane_b32 s70, v81, 42\n\t" \
    "v_readlane_b32 s71, v81, 43\n\t" \
    "v_readlane_b32 s72, v81, 44\n\t" \
    "v_readlane_b32 s73, v81, 45\n\t" \
    "v_readlane_b32 s74, v81, 46\n\t" \
    "v_readlane_b32 s75, v81, 47\n\t" \
    "v_mbcnt_lo_u32_b32 v100, s68, 0\n\t" \
    "v_mbcnt_hi_u32_b32 v100, s69, v100\n\t" \
    "s_bcnt1_i32_b64 s87, s[68:69]\n\t" \
    "v_lshlrev_b32 v100, 2, v100\n\t" \
    "s_lshl2_add_u32 s86, s87, 0\n\t" \
    "ds_bpermute_b32 v22, v100, v81\n\t" \
    "v_mbcnt_lo_u32_b32 v101, s70, 0\n\t" \
    "v_mbcnt_hi_u32_b32 v101, s71, v101\n\t" \
    "s_bcnt1_i32_b64 s87, s[70:71]\n\t" \
    "v_lshl_add_u32 v101, v101, 2, s86\n\t" \
    "s_lshl2_add_u32 s86, s87, s86\n\t" \
    "ds_bpermute_b32 v26, v101, v81\n\t" \
    "v_mbcnt_lo_u32_b32 v100, s72, 0\n\t" \
    "v_mbcnt_hi_u32_b32 v100, s73, v100\n\t" \
    "s_bcnt1_i32_b64 s87, s[72:73]\n\t" \
    "v_lshl_add_u32 v100, v100, 2, s86\n\t" \
    "s_lshl2_add_u32 s86, s87, s86\n\t" \
    "ds_bpermute_b32 v30, v100, v81\n\t" \
    "v_mbcnt_lo_u32_b32 v101, s74, 0\n\t" \
    "v_mbcnt_hi_u32_b32 v101, s75, v101\n\t" \
    "s_bcnt1_i32_b64 s87, s[74:75]\n\t" \
    "v_lshl_add_u32 v101, v101, 2, s86\n\t" \
    "s_lshl2_add_u32 s86, s87, s86\n\t" \
    "ds_bpermute_b32 v34, v101, v81\n\t" \
    "s_bitcmp0_b32 s91, 0\n\t" \
    "s_cbranch_scc1 12f\n\t" \
    "v_readlane_b32 s76, v81, 48\n\t" \
    "v_readlane_b32 s77, v81, 49\n\t" \
    "s_nop 0\n\t" \
    "v_mbcnt_lo_u32_b32 v100, s76, 0\n\t" \
    "v_mbcnt_hi_u32_b32 v100, s77, v100\n\t" \
    "v_lshl_add_u32 v100, v100, 2, s86\n\t" \
    "ds_bpermute_b32 v38, v100, v81\n\t" \
    "12:\n\t" \
    "v_readlane_b32 s68, v82, 40\n\t" \
    "v_readlane_b32 s69, v82, 41\n\t" \
    "v_readlane_b32 s70, v82, 42\n\t" \
    "v_readlane_b32 s71, v82, 43\n\t" \
    "v_readlane_b32 s72, v82, 44\n\t" \
    "v_readlane_b32 s73, v82, 45\n\t" \
    "v_readlane_b32 s74, v82, 46\n\t" \
    "v_readlane_b32 s75, v82, 47\n\t" \
    "v_mbcnt_lo_u32_b32 v100, s68, 0\n\t" \
    "v_mbcnt_hi_u32_b32 v100, s69, v100\n\t" \
    "s_bcnt1_i32_b64 s87, s[68:69]\n\t" \
    "v_lshlrev_b32 v100, 2, v100\n\t" \
    "s_lshl2_add_u32 s86, s87, 0\n\t" \
    "ds_bpermute_b32 v42, v100, v82\n\t" \
    "v_mbcnt_lo_u32_b32 v101, s70, 0\n\t" \
    "v_mbcnt_hi_u32_b32 v101, s71, v101\n\t" \
    "s_bcnt1_i32_b64 s87, s[70:71]\n\t" \
    "v_lshl_add_u32 v101, v101, 2, s86\n\t" \
    "s_lshl2_add_u32 s86, s87, s86\n\t" \
    "ds_bpermute_b32 v46, v101, v82\n\t" \
    "v_mbcnt_lo_u32_b32 v100, s72, 0\n\t" \
    "v_mbcnt_hi_u32_b32 v100, s73, v100\n\t" \
    "s_bcnt1_i32_b64 s87, s[72:73]\n\t" \
    "v_lshl_add_u32 v100, v100, 2, s86\n\t" \
    "s_lshl2_add_u32 s86, s87, s86\n\t" \
    "ds_bpermute_b32 v50, v100, v82\n\t" \
    "v_mbcnt_lo_u32_b32 v101, s74, 0\n\t" \
    "v_mbcnt_hi_u32_b32 v101, s75, v101\n\t" \
    "s_bcnt1_i32_b64 s87, s[74:75]\n\t" \
    "v_lshl_add_u32 v101, v101, 2, s86\n\t" \
    "s_lshl2_add_u32 s86, s87, s86\n\t" \
    "ds_bpermute_b32 v54, v101, v82\n\t" \
    "s_bitcmp0_b32 s91, 1\n\t" \
    "s_cbranch_scc1 13f\n\t" \
    "v_readlane_b32 s76, v82, 48\n\t" \
    "v_readlane_b32 s77, v82, 49\n\t" \
    "s_nop 0\n\t" \
    "v_mbcnt_lo_u32_b32 v100, s76, 0\n\t" \
    "v_mbcnt_hi_u32_b32 v100, s77, v100\n\t" \
    "v_lshl_add_u32 v100, v100, 2, s86\n\t" \
    "ds_bpermute_b32 v58, v100, v82\n\t" \
    "13:\n\t" \
    "s_waitcnt lgkmcnt(0)\n\t" \
    "v_and_b32 v102, 7, v83\n\t" \
    "buffer_load_dwordx4 v[6:9], v104, %4, s94 offen\n\t" \
    "s_add_u32 s87, s94, 16384\n\t" \
    "buffer_load_dwordx4 v[10:13], v104, %4, s87 offen\n\t" \
    "s_add_u32 s87, s94, 32768\n\t" \
    "buffer_load_dwordx4 v[14:17], v104, %4, s87 offen\n\t" \
    "s_add_u32 s87, s94, 49152\n\t" \
    "buffer_load_dwordx2 v[18:19], v105, %4, s87 offen\n\t" \
    "s_add_u32 s94, s94, 57152\n\t" \
    "buffer_load_dwordx4 v[80:83], v104, %4, s94 offen\n\t" \
    "s_add_u32 s87, s94, 16384\n\t" \
    "buffer_load_dwordx4 v[84:87], v104, %4, s87 offen\n\t" \
    "s_add_u32 s87, s94, 32768\n\t" \
    "buffer_load_dwordx4 v[88:91], v104, %4, s87 offen\n\t" \
    "s_add_u32 s87, s94, 49152\n\t" \
    "buffer_load_dwordx2 v[98:99], v105, %4, s87 offen\n\t" \
    "s_add_u32 s94, s94, 57152\n\t" \
    "s_branch 14f\n\t" \
    ".p2align 12\n\t" \
    "kw%c14_1:\n\t" \
    "s_setpc_b64 vcc\n\t" \
    "14:\n\t" \
    "s_lshr_b32 s87, %12, 8\n\t" \
    "s_or_b32 s87, s87, %8\n\t" \
    "s_cbranch_scc1 6f\n\t" \
    "s_and_b32 s87, %12, 15\n\t" \
    "s_lshl_b32 s87, s87, 8\n\t" \
    "v_lshl_add_u32 v100, v102, 2, s87\n\t" \
    "s_mov_b32 s86, 0x400000\n\t" \
    "5:\n\t" \
    "global_load_dword v92, v100, %11 offset:128 sc1\n\t" \
    "s_waitcnt vmcnt(0)\n\t" \
    "v_cmp_ne_u32 vcc, 1, v92\n\t" \
    "s_cmp_eq_u64 vcc, 0\n\t" \
    "s_cbranch_scc1 4f\n\t" \
    "s_sleep 4\n\t" \
    "s_sub_u32 s86, s86, 1\n\t" \
    "s_cmp_lg_u32 s86, 0\n\t" \
    "s_cbranch_scc1 5b\n\t" \
    "4:\n\t" \
    "v_mov_b32 v100, s86\n\t" \
    "v_mov_b32 v101, 163808\n\t" \
    "ds_write_b32 v101, v100\n\t" \
    "s_waitcnt lgkmcnt(0)\n\t" \
    "6:\n\t" \
    "s_barrier\n\t" \
    "7:\n\t" \
    "s_cmp_eq_u32 %6, 0\n\t" \
    "s_cbranch_scc1 9f\n\t" \
    "s_setprio 3\n\t" \
    "v_add_u32 v22, v22, v103\n\t" \
    "v_and_b32 v22, -4, v22\n\t" \
    "buffer_load_dwordx3 v[20:22], v22, %5, 0 offen offset:0 sc1\n\t" \
    "v_add_u32 v26, v26, v103\n\t" \
    "v_and_b32 v26, -4, v26\n\t" \
    "buffer_load_dwordx3 v[24:26], v26, %5, 0 offen offset:640 sc1\n\t" \
    "v_add_u32 v30, v30, v103\n\t" \
    "v_and_b32 v30, -4, v30\n\t" \
    "buffer_load_dwordx3 v[28:30], v30, %5, 0 offen offset:1280 sc1\n\t" \
    "v_add_u32 v34, v34, v103\n\t" \
    "v_and_b32 v34, -4, v34\n\t" \
    "buffer_load_dwordx3 v[32:34], v34, %5, 0 offen offset:1920 sc1\n\t" \
    "s_bitcmp0_b32 s91, 0\n\t" \
    "s_cbranch_scc1 15f\n\t" \
    "v_add_u32 v38, v38, v103\n\t" \
    "v_and_b32 v38, -4, v38\n\t" \
    "buffer_load_dwordx3 v[36:38], v38, %5, 0 offen offset:2560 sc1\n\t" \
    "15:\n\t" \
    "s_setprio 2\n\t" \
    "v_mov_b32 v101, 163808\n\t" \
    "ds_read_b32 v100, v101\n\t" \
    "s_waitcnt lgkmcnt(0)\n\t" \
    "v_readfirstlane_b32 s87, v100\n\t" \
    "s_cmp_eq_u32 s87, 0\n\t" \
    "s_cbranch_scc1 9f\n\t" \
    "s_waitcnt vmcnt(3)\n\t" \
    "v_bfe_i32 v100, v21, 5, 1\n\t" \
    "v_bfi_b32 v101, v100, s88, v114\n\t" \
    "v_perm_b32 v101, v22, v20, v101\n\t" \
    "v_bfi_b32 v20, v100, v20, v22\n\t" \
    "v_perm_b32 v100, v21, v20, s89\n\t" \
    "v_lshlrev_b32 v100, 3, v100\n\t" \
    "v_bfi_b32 v22, s90, v100, v101\n\t" \
    "s_waitcnt vmcnt(2)\n\t" \
    "v_bfe_i32 v100, v25, 5, 1\n\t" \
    "v_bfi_b32 v101, v100, s88, v114\n\t" \
    "v_perm_b32 v101, v26, v24, v101\n\t" \
    "v_bfi_b32 v24, v100, v24, v26\n\t" \
    "v_perm_b32 v100, v25, v24, s89\n\t" \
    "v_lshlrev_b32 v100, 3, v100\n\t" \
    "v_bfi_b32 v26, s90, v100, v101\n\t" \
    "s_waitcnt vmcnt(1)\n\t" \
    "v_bfe_i32 v100, v29, 5, 1\n\t" \
    "v_bfi_b32 v101, v100, s88, v114\n\t" \
    "v_perm_b32 v101, v30, v28, v101\n\t" \
    "v_bfi_b32 v28, v100, v28, v30\n\t" \
    "v_perm_b32 v100, v29, v28, s89\n\t" \
    "v_lshlrev_b32 v100, 3, v100\n\t" \
    "v_bfi_b32 v30, s90, v100, v101\n\t" \
    "s_waitcnt vmcnt(0)\n\t" \
    "v_bfe_i32 v100, v33, 5, 1\n\t" \
    "v_bfi_b32 v101, v100, s88, v114\n\t" \
    "v_perm_b32 v101, v34, v32, v101\n\t" \
    "v_bfi_b32 v32, v100, v32, v34\n\t" \
    "v_perm_b32 v100, v33, v32, s89\n\t" \
    "v_lshlrev_b32 v100, 3, v100\n\t" \
    "v_bfi_b32 v34, s90, v100, v101\n\t" \
    "v_and_b32 v23, 0xffff, v22\n\t" \
    "v_lshrrev_b32 v27, 16, v22\n\t" \
    "v_cmp_nlt_f32 s[48:49], |v20|, 2.0\n\t" \
    "v_cmp_nlt_f32 s[50:51], |v21|, 2.0\n\t" \
    "v_and_b32 v31, 0xffff, v26\n\t" \
    "v_lshrrev_b32 v35, 16, v26\n\t" \
    "v_cmp_nlt_f32 s[52:53], |v24|, 2.0\n\t" \
    "v_cmp_nlt_f32 s[54:55], |v25|, 2.0\n\t" \
    "v_and_b32 v39, 0xffff, v30\n\t" \
    "v_lshrrev_b32 v43, 16, v30\n\t" \
    "v_cmp_nlt_f32 s[56:57], |v28|, 2.0\n\t" \
    "v_cmp_nlt_f32 s[58:59], |v29|, 2.0\n\t" \
    "v_and_b32 v47, 0xffff, v34\n\t" \
    "v_lshrrev_b32 v51, 16, v34\n\t" \
    "v_cmp_nlt_f32 s[60:61], |v32|, 2.0\n\t" \
    "v_cmp_nlt_f32 s[62:63], |v33|, 2.0\n\t" \
    "s_bitcmp0_b32 s91, 0\n\t" \
    "s_cbranch_scc1 16f\n\t" \
    "s_waitcnt vmcnt(0)\n\t" \
    "v_bfe_i32 v100, v37, 5, 1\n\t" \
    "v_bfi_b32 v101, v100, s88, v114\n\t" \
    "v_perm_b32 v101, v38, v36, v101\n\t" \
    "v_bfi_b32 v36, v100, v36, v38\n\t" \
    "v_perm_b32 v100, v37, v36, s89\n\t" \
    "v_lshlrev_b32 v100, 3, v100\n\t" \
    "v_bfi_b32 v38, s90, v100, v101\n\t" \
    "v_and_b32 v55, 0xffff, v38\n\t" \
    "v_lshrrev_b32 v59, 16, v38\n\t" \
    "v_cmp_nlt_f32 s[64:65], |v36|, 2.0\n\t" \
    "v_cmp_nlt_f32 s[66:67], |v37|, 2.0\n\t" \
    "16:\n\t" \
    "s_setprio 2\n\t" \
    "s_not_b64 exec, s[48:49]\n\t" \
    "ds_read_b64 v[60:61], v23 offset:0\n\t" \
    "s_not_b64 exec, s[50:51]\n\t" \
    "ds_read_b64 v[62:63], v27 offset:0\n\t" \
    "s_not_b64 exec, s[52:53]\n\t" \
    "ds_read_b64 v[64:65], v31 offset:0\n\t" \
    "s_not_b64 exec, s[54:55]\n\t" \
    "ds_read_b64 v[66:67], v35 offset:0\n\t" \
    "s_not_b64 exec, s[56:57]\n\t" \
    "ds_read_b64 v[68:69], v39 offset:0\n\t" \
    "s_not_b64 exec, s[58:59]\n\t" \
    "ds_read_b64 v[70:71], v43 offset:0\n\t" \
    "s_not_b64 exec, s[60:61]\n\t" \
    "ds_read_b64 v[72:73], v47 offset:0\n\t" \
    "s_not_b64 exec, s[62:63]\n\t" \
    "ds_read_b64 v[74:75], v51 offset:0\n\t" \
    "s_bitcmp0_b32 s91, 0\n\t" \
    "s_cbranch_scc1 17f\n\t" \
    "s_not_b64 exec, s[64:65]\n\t" \
    "ds_read_b64 v[76:77], v55 offset:0\n\t" \
    "s_not_b64 exec, s[66:67]\n\t" \
    "ds_read_b64 v[78:79], v59 offset:0\n\t" \
    "17:\n\t" \
    "s_mov_b64 exec, -1\n\t" \
    "s_setprio 0\n\t" \
    "v_add_u32 v42, v42, v103\n\t" \
    "v_and_b32 v42, -4, v42\n\t" \
    "buffer_load_dwordx3 v[40:42], v42, %5, 0 offen offset:0 sc1\n\t" \
    "v_add_u32 v46, v46, v103\n\t" \
    "v_and_b32 v46, -4, v46\n\t" \
    "buffer_load_dwordx3 v[44:46], v46, %5, 0 offen offset:640 sc1\n\t" \
    "s_waitcnt lgkmcnt(0)\n\t" \
    "s_barrier\n\t" \
    "s_setprio 3\n\t" \
    "ds_write_b128 v104, v[6:9] offset:0\n\t" \
    "ds_write_b128 v104, v[10:13] offset:16384\n\t" \
    "s_setprio 2\n\t" \
    "ds_write_b128 v104, v[14:17] offset:32768\n\t" \
    "s_mov_b64 exec, s[80:81]\n\t" \
    "ds_write_b64 v105, v[18:19] offset:49152\n\t" \
    "s_mov_b64 exec, -1\n\t" \
    "s_setprio 1\n\t" \
    "s_waitcnt lgkmcnt(0)\n\t" \
    "s_barrier\n\t" \
    "s_setprio 3\n\t" \
    "s_mov_b64 exec, s[48:49]\n\t" \
    "ds_read_b64 v[60:61], v23 offset:0\n\t" \
    "s_mov_b64 exec, s[50:51]\n\t" \
    "ds_read_b64 v[62:63], v27 offset:0\n\t" \
    "s_mov_b64 exec, s[52:53]\n\t" \
    "ds_read_b64 v[64:65], v31 offset:0\n\t" \
    "s_mov_b64 exec, s[54:55]\n\t" \
    "ds_read_b64 v[66:67], v35 offset:0\n\t" \
    "s_mov_b64 exec, -1\n\t" \
    "v_add_u32 v50, v50, v103\n\t" \
    "v_and_b32 v50, -4, v50\n\t" \
    "buffer_load_dwordx3 v[48:50], v50, %5, 0 offen offset:1280 sc1\n\t" \
    "v_readlane_b32 s68, v107, 40\n\t" \
    "v_readlane_b32 s69, v107, 41\n\t" \
    "v_readlane_b32 s70, v107, 42\n\t" \
    "v_readlane_b32 s71, v107, 43\n\t" \
    "v_readlane_b32 s72, v107, 44\n\t" \
    "v_readlane_b32 s73, v107, 45\n\t" \
    "v_readlane_b32 s74, v107, 46\n\t" \
    "v_readlane_b32 s75, v107, 47\n\t" \
    "v_mbcnt_lo_u32_b32 v100, s68, 0\n\t" \
    "v_mbcnt_hi_u32_b32 v100, s69, v100\n\t" \
    "s_bcnt1_i32_b64 s87, s[68:69]\n\t" \
    "v_lshlrev_b32 v100, 2, v100\n\t" \
    "s_lshl2_add_u32 s86, s87, 0\n\t" \
    "ds_bpermute_b32 v22, v100, v107\n\t" \
    "v_mbcnt_lo_u32_b32 v101, s70, 0\n\t" \
    "v_mbcnt_hi_u32_b32 v101, s71, v101\n\t" \
    "s_bcnt1_i32_b64 s87, s[70:71]\n\t" \
    "v_lshl_add_u32 v101, v101, 2, s86\n\t" \
    "s_lshl2_add_u32 s86, s87, s86\n\t" \
    "ds_bpermute_b32 v26, v101, v107\n\t" \
    "v_mbcnt_lo_u32_b32 v100, s72, 0\n\t" \
    "v_mbcnt_hi_u32_b32 v100, s73, v100\n\t" \
    "s_bcnt1_i32_b64 s87, s[72:73]\n\t" \
    "v_lshl_add_u32 v100, v100, 2, s86\n\t" \
    "s_lshl2_add_u32 s86, s87, s86\n\t" \
    "ds_bpermute_b32 v30, v100, v107\n\t" \
    "v_mbcnt_lo_u32_b32 v101, s74, 0\n\t" \
    "v_mbcnt_hi_u32_b32 v101, s75, v101\n\t" \
    "s_bcnt1_i32_b64 s87, s[74:75]\n\t" \
    "v_lshl_add_u32 v101, v101, 2, s86\n\t" \
    "s_lshl2_add_u32 s86, s87, s86\n\t" \
    "ds_bpermute_b32 v34, v101, v107\n\t" \
    "s_bitcmp0_b32 s91, 2\n\t" \
    "s_cbranch_scc1 18f\n\t" \
    "v_readlane_b32 s76, v107, 48\n\t" \
    "v_readlane_b32 s77, v107, 49\n\t" \
    "s_nop 0\n\t" \
    "v_mbcnt_lo_u32_b32 v100, s76, 0\n\t" \
    "v_mbcnt_hi_u32_b32 v100, s77, v100\n\t" \
    "v_lshl_add_u32 v100, v100, 2, s86\n\t" \
    "ds_bpermute_b32 v38, v100, v107\n\t" \
    "18:\n\t" \
    "s_setprio 2\n\t" \
    "s_mov_b64 exec, s[56:57]\n\t" \
    "ds_read_b64 v[68:69], v39 offset:0\n\t" \
    "s_mov_b64 exec, s[58:59]\n\t" \
    "ds_read_b64 v[70:71], v43 offset:0\n\t" \
    "s_mov_b64 exec, s[60:61]\n\t" \
    "ds_read_b64 v[72:73], v47 offset:0\n\t" \
    "s_mov_b64 exec, s[62:63]\n\t" \
    "ds_read_b64 v[74:75], v51 offset:0\n\t" \
    "s_bitcmp0_b32 s91, 0\n\t" \
    "s_cbranch_scc1 19f\n\t" \
    "s_mov_b64 exec, s[64:65]\n\t" \
    "ds_read_b64 v[76:77], v55 offset:0\n\t" \
    "s_mov_b64 exec, s[66:67]\n\t" \
    "ds_read_b64 v[78:79], v59 offset:0\n\t" \
    "19:\n\t" \
    "s_mov_b64 exec, -1\n\t" \
    "s_setprio 1\n\t" \
    "v_add_u32 v54, v54, v103\n\t" \
    "v_and_b32 v54, -4, v54\n\t" \
    "buffer_load_dwordx3 v[52:54], v54, %5, 0 offen offset:1920 sc1\n\t" \
    "s_bitcmp0_b32 s91, 1\n\t" \
    "s_cbranch_scc1 20f\n\t" \
    "v_add_u32 v58, v58, v103\n\t" \
    "v_and_b32 v58, -4, v58\n\t" \
    "buffer_load_dwordx3 v[56:58], v58, %5, 0 offen offset:2560 sc1\n\t" \
    "20:\n\t" \
    "s_waitcnt vmcnt(3)\n\t" \
    "v_bfe_i32 v100, v41, 5, 1\n\t" \
    "v_bfi_b32 v101, v100, s88, v114\n\t" \
    "v_perm_b32 v101, v42, v40, v101\n\t" \
    "v_bfi_b32 v40, v100, v40, v42\n\t" \
    "v_perm_b32 v100, v41, v40, s89\n\t" \
    "v_lshlrev_b32 v100, 3, v100\n\t" \
    "v_bfi_b32 v42, s90, v100, v101\n\t" \
    "s_waitcnt vmcnt(2)\n\t" \
    "v_bfe_i32 v100, v45, 5, 1\n\t" \
    "v_bfi_b32 v101, v100, s88, v114\n\t" \
    "v_perm_b32 v101, v46, v44, v101\n\t" \
    "v_bfi_b32 v44, v100, v44, v46\n\t" \
    "v_perm_b32 v100, v45, v44, s89\n\t" \
    "v_lshlrev_b32 v100, 3, v100\n\t" \
    "v_bfi_b32 v46, s90, v100, v101\n\t" \
    "s_waitcnt lgkmcnt(0)\n\t" \
    "s_barrier\n\t" \
    "s_mov_b64 exec, s[80:81]\n\t" \
    "ds_write_b64 v105, v[98:99] offset:49152\n\t" \
    "s_mov_b64 exec, -1\n\t" \
    "s_setprio 3\n\t" \
    "ds_write_b128 v104, v[80:83] offset:0\n\t" \
    "s_waitcnt vmcnt(1)\n\t" \
    "v_bfe_i32 v100, v49, 5, 1\n\t" \
    "v_bfi_b32 v101, v100, s88, v114\n\t" \
    "v_perm_b32 v101, v50, v48, v101\n\t" \
    "v_bfi_b32 v48, v100, v48, v50\n\t" \
    "v_perm_b32 v100, v49, v48, s89\n\t" \
    "v_lshlrev_b32 v100, 3, v100\n\t" \
    "v_bfi_b32 v50, s90, v100, v101\n\t" \
    "v_ashrrev_i32 v101, 3, v20\n\t" \
    "v_lshrrev_b32 v100, 2, v20\n\t" \
    "v_and_b32 v101, 0x87ff0000, v101\n\t" \
    "s_mov_b64 vcc, s[82:83]\n\t" \
    "v_cndmask_b32_dpp v93, v61, v60, vcc quad_perm:[1,0,3,2] row_mask:0xf bank_mask:0xf\n\t" \
    "s_not_b64 vcc, vcc\n\t" \
    "v_and_b32 v100, 0x1fff0, v100\n\t" \
    "v_cndmask_b32_dpp v95, v60, v61, vcc quad_perm:[1,0,3,2] row_mask:0xf bank_mask:0xf\n\t" \
    "v_mul_f32_dpp v98, v101, v113 quad_perm:[0,0,2,2] row_mask:0xf bank_mask:0xf\n\t" \
    "v_mul_f32_dpp v99, v101, v113 quad_perm:[1,1,3,3] row_mask:0xf bank_mask:0xf\n\t" \
    "v_lshlrev_b32 v92, 16, v93\n\t" \
    "v_and_b32 v93, 0xffff0000, v93\n\t" \
    "v_lshlrev_b32 v94, 16, v95\n\t" \
    "v_and_b32 v95, 0xffff0000, v95\n\t" \
    "v_pk_fma_f32 v[92:93], v[92:93], v[98:99], s[78:79] op_sel:[0,0,0] op_sel_hi:[1,0,1]\n\t" \
    "v_pk_fma_f32 v[94:95], v[94:95], v[98:99], s[78:79] op_sel:[0,1,0] op_sel_hi:[1,1,1]\n\t" \
    "v_add_u32_dpp v96, v100, v112 quad_perm:[0,0,2,2] row_mask:0xf bank_mask:0xf\n\t" \
    "v_add_u32_dpp v97, v100, v112 quad_perm:[1,1,3,3] row_mask:0xf bank_mask:0xf\n\t" \
    "v_add_u32 v93, s84, v93\n\t" \
    "v_add_u32 v95, s84, v95\n\t" \
    "ds_add_u64 v96, v[92:93]\n\t" \
    "ds_add_u64 v97, v[94:95]\n\t" \
    "buffer_load_dwordx4 v[6:9], v104, %4, s94 offen\n\t" \
    "ds_write_b128 v104, v[84:87] offset:16384\n\t" \
    "s_setprio 2\n\t" \
    "s_add_u32 s87, s94, 16384\n\t" \
    "buffer_load_dwordx4 v[10:13], v104, %4, s87 offen\n\t" \
    "s_bitcmp0_b32 s91, 0\n\t" \
    "s_cbranch_scc1 21f\n\t" \
    "v_ashrrev_i32 v101, 3, v36\n\t" \
    "v_lshrrev_b32 v100, 2, v36\n\t" \
    "v_and_b32 v101, 0x87ff0000, v101\n\t" \
    "s_mov_b64 vcc, s[82:83]\n\t" \
    "v_cndmask_b32_dpp v93, v77, v76, vcc quad_perm:[1,0,3,2] row_mask:0xf bank_mask:0xf\n\t" \
    "s_not_b64 vcc, vcc\n\t" \
    "v_and_b32 v100, 0x1fff0, v100\n\t" \
    "v_cndmask_b32_dpp v95, v76, v77, vcc quad_perm:[1,0,3,2] row_mask:0xf bank_mask:0xf\n\t" \
    "v_mul_f32_dpp v98, v101, v113 quad_perm:[0,0,2,2] row_mask:0xf bank_mask:0xf\n\t" \
    "v_mul_f32_dpp v99, v101, v113 quad_perm:[1,1,3,3] row_mask:0xf bank_mask:0xf\n\t" \
    "v_lshlrev_b32 v92, 16, v93\n\t" \
    "v_and_b32 v93, 0xffff0000, v93\n\t" \
    "v_lshlrev_b32 v94, 16, v95\n\t" \
    "v_and_b32 v95, 0xffff0000, v95\n\t" \
    "v_pk_fma_f32 v[92:93], v[92:93], v[98:99], s[78:79] op_sel:[0,0,0] op_sel_hi:[1,0,1]\n\t" \
    "v_pk_fma_f32 v[94:95], v[94:95], v[98:99], s[78:79] op_sel:[0,1,0] op_sel_hi:[1,1,1]\n\t" \
    "v_add_u32_dpp v96, v100, v112 quad_perm:[0,0,2,2] row_mask:0xf bank_mask:0xf\n\t" \
    "v_add_u32_dpp v97, v100, v112 quad_perm:[1,1,3,3] row_mask:0xf bank_mask:0xf\n\t" \
    "v_add_u32 v93, s84, v93\n\t" \
    "v_add_u32 v95, s84, v95\n\t" \
    "v_cmpx_ne_u32 vcc, 0, v98\n\t" \
    "ds_add_u64 v96, v[92:93]\n\t" \
    "s_mov_b64 exec, -1\n\t" \
    "v_cmpx_ne_u32 vcc, 0, v99\n\t" \
    "ds_add_u64 v97, v[94:95]\n\t" \
    "s_mov_b64 exec, -1\n\t" \
    "v_ashrrev_i32 v101, 3, v37\n\t" \
    "v_lshrrev_b32 v100, 2, v37\n\t" \
    "v_and_b32 v101, 0x87ff0000, v101\n\t" \
    "s_mov_b64 vcc, s[82:83]\n\t" \
    "v_cndmask_b32_dpp v93, v79, v78, vcc quad_perm:[1,0,3,2] row_mask:0xf bank_mask:0xf\n\t" \
    "s_not_b64 vcc, vcc\n\t" \
    "v_and_b32 v100, 0x1fff0, v100\n\t" \
    "v_cndmask_b32_dpp v95, v78, v79, vcc quad_perm:[1,0,3,2] row_mask:0xf bank_mask:0xf\n\t" \
    "v_mul_f32_dpp v98, v101, v113 quad_perm:[0,0,2,2] row_mask:0xf bank_mask:0xf\n\t" \
    "v_mul_f32_dpp v99, v101, v113 quad_perm:[1,1,3,3] row_mask:0xf bank_mask:0xf\n\t" \
    "v_lshlrev_b32 v92, 16, v93\n\t" \
    "v_and_b32 v93, 0xffff0000, v93\n\t" \
    "v_lshlrev_b32 v94, 16, v95\n\t" \
    "v_and_b32 v95, 0xffff0000, v95\n\t" \
    "v_pk_fma_f32 v[92:93], v[92:93], v[98:99], s[78:79] op_sel:[0,0,0] op_sel_hi:[1,0,1]\n\t" \
    "v_pk_fma_f32 v[94:95], v[94:95], v[98:99], s[78:79] op_sel:[0,1,0] op_sel_hi:[1,1,1]\n\t" \
    "v_add_u32_dpp v96, v100, v112 quad_perm:[0,0,2,2] row_mask:0xf bank_mask:0xf\n\t" \
    "v_add_u32_dpp v97, v100, v112 quad_perm:[1,1,3,3] row_mask:0xf bank_mask:0xf\n\t" \
    "v_add_u32 v93, s84, v93\n\t" \
    "v_add_u32 v95, s84, v95\n\t" \
    "v_cmpx_ne_u32 vcc, 0, v98\n\t" \
    "ds_add_u64 v96, v[92:93]\n\t" \
    "s_mov_b64 exec, -1\n\t" \
    "v_cmpx_ne_u32 vcc, 0, v99\n\t" \
    "ds_add_u64 v97, v[94:95]\n\t" \
    "s_mov_b64 exec, -1\n\t" \
    "21:\n\t" \
    "s_waitcnt vmcnt(2)\n\t" \
    "v_bfe_i32 v100, v53, 5, 1\n\t" \
    "v_bfi_b32 v101, v100, s88, v114\n\t" \
    "v_perm_b32 v101, v54, v52, v101\n\t" \
    "v_bfi_b32 v52, v100, v52, v54\n\t" \
    "v_perm_b32 v100, v53, v52, s89\n\t" \
    "v_lshlrev_b32 v100, 3, v100\n\t" \
    "v_bfi_b32 v54, s90, v100, v101\n\t" \
    "v_ashrrev_i32 v101, 3, v21\n\t" \
    "v_lshrrev_b32 v100, 2, v21\n\t" \
    "v_and_b32 v101, 0x87ff0000, v101\n\t" \
    "s_mov_b64 vcc, s[82:83]\n\t" \
    "v_cndmask_b32_dpp v93, v63, v62, vcc quad_perm:[1,0,3,2] row_mask:0xf bank_mask:0xf\n\t" \
    "s_not_b64 vcc, vcc\n\t" \
    "v_and_b32 v100, 0x1fff0, v100\n\t" \
    "v_cndmask_b32_dpp v95, v62, v63, vcc quad_perm:[1,0,3,2] row_mask:0xf bank_mask:0xf\n\t" \
    "v_mul_f32_dpp v98, v101, v113 quad_perm:[0,0,2,2] row_mask:0xf bank_mask:0xf\n\t" \
    "v_mul_f32_dpp v99, v101, v113 quad_perm:[1,1,3,3] row_mask:0xf bank_mask:0xf\n\t" \
    "v_lshlrev_b32 v92, 16, v93\n\t" \
    "v_and_b32 v93, 0xffff0000, v93\n\t" \
    "v_lshlrev_b32 v94, 16, v95\n\t" \
    "v_and_b32 v95, 0xffff0000, v95\n\t" \
    "v_pk_fma_f32 v[92:93], v[92:93], v[98:99], s[78:79] op_sel:[0,0,0] op_sel_hi:[1,0,1]\n\t" \
    "v_pk_fma_f32 v[94:95], v[94:95], v[98:99], s[78:79] op_sel:[0,1,0] op_sel_hi:[1,1,1]\n\t" \
    "v_add_u32_dpp v96, v100, v112 quad_perm:[0,0,2,2] row_mask:0xf bank_mask:0xf\n\t" \
    "v_add_u32_dpp v97, v100, v112 quad_perm:[1,1,3,3] row_mask:0xf bank_mask:0xf\n\t" \
    "v_add_u32 v93, s84, v93\n\t" \
    "v_add_u32 v95, s84, v95\n\t" \
    "ds_add_u64 v96, v[92:93]\n\t" \
    "ds_add_u64 v97, v[94:95]\n\t" \
    "ds_write_b128 v104, v[88:91] offset:32768\n\t" \
    "s_setprio 1\n\t" \
    "s_add_u32 s87, s94, 32768\n\t" \
    "buffer_load_dwordx4 v[14:17], v104, %4, s87 offen\n\t" \
    "v_and_b32 v23, 0xffff, v42\n\t" \
    "v_lshrrev_b32 v27, 16, v42\n\t" \
    "v_cmp_nlt_f32 s[48:49], |v40|, 2.0\n\t" \
    "v_cmp_nlt_f32 s[50:51], |v41|, 2.0\n\t" \
    "v_and_b32 v31, 0xffff, v46\n\t" \
    "v_lshrrev_b32 v35, 16, v46\n\t" \
    "v_cmp_nlt_f32 s[52:53], |v44|, 2.0\n\t" \
    "v_cmp_nlt_f32 s[54:55], |v45|, 2.0\n\t" \
    "v_and_b32 v39, 0xffff, v50\n\t" \
    "v_lshrrev_b32 v43, 16, v50\n\t" \
    "v_cmp_nlt_f32 s[56:57], |v48|, 2.0\n\t" \
    "v_cmp_nlt_f32 s[58:59], |v49|, 2.0\n\t" \
    "v_and_b32 v47, 0xffff, v54\n\t" \
    "v_lshrrev_b32 v51, 16, v54\n\t" \
    "v_cmp_nlt_f32 s[60:61], |v52|, 2.0\n\t" \
    "v_cmp_nlt_f32 s[62:63], |v53|, 2.0\n\t" \
    "s_bitcmp0_b32 s91, 1\n\t" \
    "s_cbranch_scc1 22f\n\t" \
    "s_waitcnt vmcnt(3)\n\t" \
    "v_bfe_i32 v100, v57, 5, 1\n\t" \
    "v_bfi_b32 v101, v100, s88, v114\n\t" \
    "v_perm_b32 v101, v58, v56, v101\n\t" \
    "v_bfi_b32 v56, v100, v56, v58\n\t" \
    "v_perm_b32 v100, v57, v56, s89\n\t" \
    "v_lshlrev_b32 v100, 3, v100\n\t" \
    "v_bfi_b32 v58, s90, v100, v101\n\t" \
    "v_and_b32 v55, 0xffff, v58\n\t" \
    "v_lshrrev_b32 v59, 16, v58\n\t" \
    "v_cmp_nlt_f32 s[64:65], |v56|, 2.0\n\t" \
    "v_cmp_nlt_f32 s[66:67], |v57|, 2.0\n\t" \
    "22:\n\t" \
    "v_add_u32 v22, v22, v103\n\t" \
    "v_and_b32 v22, -4, v22\n\t" \
    "buffer_load_dwordx3 v[20:22], v22, %5, 0 offen offset:0 sc1\n\t" \
    "s_setprio 0\n\t" \
    "v_ashrrev_i32 v101, 3, v24\n\t" \
    "v_lshrrev_b32 v100, 2, v24\n\t" \
    "v_and_b32 v101, 0x87ff0000, v101\n\t" \
    "s_mov_b64 vcc, s[82:83]\n\t" \
    "v_cndmask_b32_dpp v93, v65, v64, vcc quad_perm:[1,0,3,2] row_mask:0xf bank_mask:0xf\n\t" \
    "s_not_b64 vcc, vcc\n\t" \
    "v_and_b32 v100, 0x1fff0, v100\n\t" \
    "v_cndmask_b32_dpp v95, v64, v65, vcc quad_perm:[1,0,3,2] row_mask:0xf bank_mask:0xf\n\t" \
    "v_mul_f32_dpp v98, v101, v113 quad_perm:[0,0,2,2] row_mask:0xf bank_mask:0xf\n\t" \
    "v_mul_f32_dpp v99, v101, v113 quad_perm:[1,1,3,3] row_mask:0xf bank_mask:0xf\n\t" \
    "v_lshlrev_b32 v92, 16, v93\n\t" \
    "v_and_b32 v93, 0xffff0000, v93\n\t" \
    "v_lshlrev_b32 v94, 16, v95\n\t" \
    "v_and_b32 v95, 0xffff0000, v95\n\t" \
    "v_pk_fma_f32 v[92:93], v[92:93], v[98:99], s[78:79] op_sel:[0,0,0] op_sel_hi:[1,0,1]\n\t" \
    "v_pk_fma_f32 v[94:95], v[94:95], v[98:99], s[78:79] op_sel:[0,1,0] op_sel_hi:[1,1,1]\n\t" \
    "v_add_u32_dpp v96, v100, v112 quad_perm:[0,0,2,2] row_mask:0xf bank_mask:0xf\n\t" \
    "v_add_u32_dpp v97, v100, v112 quad_perm:[1,1,3,3] row_mask:0xf bank_mask:0xf\n\t" \
    "v_add_u32 v93, s84, v93\n\t" \
    "v_add_u32 v95, s84, v95\n\t" \
    "ds_add_u64 v96, v[92:93]\n\t" \
    "ds_add_u64 v97, v[94:95]\n\t" \
    "s_lshr_b32 s91, s91, 1\n\t" \
    "s_waitcnt lgkmcnt(2)\n\t" \
    "s_mov_b32 s92, 3\n\t" \
    "s_branch 24f\n\t" \
    ".p2align 12\n\t" \
    "kw%c14_2:\n\t" \
    "s_setpc_b64 vcc\n\t" \
    "24:\n\t" \
    "23:\n\t" \
    "s_barrier\n\t" \
    "s_setprio 3\n\t" \
    "s_not_b64 exec, s[48:49]\n\t" \
    "ds_read_b64 v[60:61], v23 offset:0\n\t" \
    "s_not_b64 exec, s[50:51]\n\t" \
    "ds_read_b64 v[62:63], v27 offset:0\n\t" \
    "s_not_b64 exec, s[52:53]\n\t" \
    "ds_read_b64 v[80:81], v31 offset:0\n\t" \
    "s_not_b64 exec, s[54:55]\n\t" \
    "ds_read_b64 v[82:83], v35 offset:0\n\t" \
    "s_mov_b64 exec, -1\n\t" \
    "v_ashrrev_i32 v101, 3, v25\n\t" \
    "v_lshrrev_b32 v100, 2, v25\n\t" \
    "v_and_b32 v101, 0x87ff0000, v101\n\t" \
    "s_mov_b64 vcc, s[82:83]\n\t" \
    "v_cndmask_b32_dpp v93, v67, v66, vcc quad_perm:[1,0,3,2] row_mask:0xf bank_mask:0xf\n\t" \
    "s_not_b64 vcc, vcc\n\t" \
    "v_and_b32 v100, 0x1fff0, v100\n\t" \
    "v_cndmask_b32_dpp v95, v66, v67, vcc quad_perm:[1,0,3,2] row_mask:0xf bank_mask:0xf\n\t" \
    "v_mul_f32_dpp v98, v101, v113 quad_perm:[0,0,2,2] row_mask:0xf bank_mask:0xf\n\t" \
    "v_mul_f32_dpp v99, v101, v113 quad_perm:[1,1,3,3] row_mask:0xf bank_mask:0xf\n\t" \
    "v_lshlrev_b32 v92, 16, v93\n\t" \
    "v_and_b32 v93, 0xffff0000, v93\n\t" \
    "v_lshlrev_b32 v94, 16, v95\n\t" \
    "v_and_b32 v95, 0xffff0000, v95\n\t" \
    "v_pk_fma_f32 v[92:93], v[92:93], v[98:99], s[78:79] op_sel:[0,0,0] op_sel_hi:[1,0,1]\n\t" \
    "v_pk_fma_f32 v[94:95], v[94:95], v[98:99], s[78:79] op_sel:[0,1,0] op_sel_hi:[1,1,1]\n\t" \
    "v_add_u32_dpp v96, v100, v112 quad_perm:[0,0,2,2] row_mask:0xf bank_mask:0xf\n\t" \
    "v_add_u32_dpp v97, v100, v112 quad_perm:[1,1,3,3] row_mask:0xf bank_mask:0xf\n\t" \
    "v_add_u32 v93, s84, v93\n\t" \
    "v_add_u32 v95, s84, v95\n\t" \
    "v_cmpx_ne_u32 vcc, 0, v98\n\t" \
    "ds_add_u64 v96, v[92:93]\n\t" \
    "s_mov_b64 exec, -1\n\t" \
    "v_cmpx_ne_u32 vcc, 0, v99\n\t" \
    "ds_add_u64 v97, v[94:95]\n\t" \
    "s_mov_b64 exec, -1\n\t" \
    "s_setprio 2\n\t" \
    "s_not_b64 exec, s[56:57]\n\t" \
    "ds_read_b64 v[84:85], v39 offset:0\n\t" \
    "s_not_b64 exec, s[58:59]\n\t" \
    "ds_read_b64 v[86:87], v43 offset:0\n\t" \
    "s_not_b64 exec, s[60:61]\n\t" \
    "ds_read_b64 v[88:89], v47 offset:0\n\t" \
    "s_not_b64 exec, s[62:63]\n\t" \
    "ds_read_b64 v[90:91], v51 offset:0\n\t" \
    "s_bitcmp0_b32 s91, 0\n\t" \
    "s_cbranch_scc1 25f\n\t" \
    "s_not_b64 exec, s[64:65]\n\t" \
    "ds_read_b64 v[76:77], v55 offset:0\n\t" \
    "s_not_b64 exec, s[66:67]\n\t" \
    "ds_read_b64 v[78:79], v59 offset:0\n\t" \
    "25:\n\t" \
    "s_mov_b64 exec, -1\n\t" \
    "s_add_u32 s87, s94, 49152\n\t" \
    "buffer_load_dwordx2 v[18:19], v105, %4, s87 offen\n\t" \
    "s_add_u32 s94, s94, 57152\n\t" \
    "v_add_u32 v26, v26, v103\n\t" \
    "v_and_b32 v26, -4, v26\n\t" \
    "buffer_load_dwordx3 v[24:26], v26, %5, 0 offen offset:640 sc1\n\t" \
    "s_waitcnt vmcnt(2)\n\t" \
    "v_bfe_i32 v100, v21, 5, 1\n\t" \
    "v_bfi_b32 v101, v100, s88, v114\n\t" \
    "v_perm_b32 v101, v22, v20, v101\n\t" \
    "v_bfi_b32 v20, v100, v20, v22\n\t" \
    "v_perm_b32 v100, v21, v20, s89\n\t" \
    "v_lshlrev_b32 v100, 3, v100\n\t" \
    "v_bfi_b32 v22, s90, v100, v101\n\t" \
    "s_setprio 1\n\t" \
    "v_ashrrev_i32 v101, 3, v28\n\t" \
    "v_lshrrev_b32 v100, 2, v28\n\t" \
    "v_and_b32 v101, 0x87ff0000, v101\n\t" \
    "s_mov_b64 vcc, s[82:83]\n\t" \
    "v_cndmask_b32_dpp v93, v69, v68, vcc quad_perm:[1,0,3,2] row_mask:0xf bank_mask:0xf\n\t" \
    "s_not_b64 vcc, vcc\n\t" \
    "v_and_b32 v100, 0x1fff0, v100\n\t" \
    "v_cndmask_b32_dpp v95, v68, v69, vcc quad_perm:[1,0,3,2] row_mask:0xf bank_mask:0xf\n\t" \
    "v_mul_f32_dpp v98, v101, v113 quad_perm:[0,0,2,2] row_mask:0xf bank_mask:0xf\n\t" \
    "v_mul_f32_dpp v99, v101, v113 quad_perm:[1,1,3,3] row_mask:0xf bank_mask:0xf\n\t" \
    "v_lshlrev_b32 v92, 16, v93\n\t" \
    "v_and_b32 v93, 0xffff0000, v93\n\t" \
    "v_lshlrev_b32 v94, 16, v95\n\t" \
    "v_and_b32 v95, 0xffff0000, v95\n\t" \
    "v_pk_fma_f32 v[92:93], v[92:93], v[98:99], s[78:79] op_sel:[0,0,0] op_sel_hi:[1,0,1]\n\t" \
    "v_pk_fma_f32 v[94:95], v[94:95], v[98:99], s[78:79] op_sel:[0,1,0] op_sel_hi:[1,1,1]\n\t" \
    "v_add_u32_dpp v96, v100, v112 quad_perm:[0,0,2,2] row_mask:0xf bank_mask:0xf\n\t" \
    "v_add_u32_dpp v97, v100, v112 quad_perm:[1,1,3,3] row_mask:0xf bank_mask:0xf\n\t" \
    "v_add_u32 v93, s84, v93\n\t" \
    "v_add_u32 v95, s84, v95\n\t" \
    "v_cmpx_ne_u32 vcc, 0, v98\n\t" \
    "ds_add_u64 v96, v[92:93]\n\t" \
    "s_mov_b64 exec, -1\n\t" \
    "v_cmpx_ne_u32 vcc, 0, v99\n\t" \
    "ds_add_u64 v97, v[94:95]\n\t" \
    "s_mov_b64 exec, -1\n\t" \
    "s_waitcnt lgkmcnt(2)\n\t" \
    "s_barrier\n\t" \
    "s_setprio 3\n\t" \
    "ds_write_b128 v104, v[6:9] offset:0\n\t" \
    "ds_write_b128 v104, v[10:13] offset:16384\n\t" \
    "v_ashrrev_i32 v101, 3, v29\n\t" \
    "v_lshrrev_b32 v100, 2, v29\n\t" \
    "v_and_b32 v101, 0x87ff0000, v101\n\t" \
    "s_mov_b64 vcc, s[82:83]\n\t" \
    "v_cndmask_b32_dpp v93, v71, v70, vcc quad_perm:[1,0,3,2] row_mask:0xf bank_mask:0xf\n\t" \
    "s_not_b64 vcc, vcc\n\t" \
    "v_and_b32 v100, 0x1fff0, v100\n\t" \
    "v_cndmask_b32_dpp v95, v70, v71, vcc quad_perm:[1,0,3,2] row_mask:0xf bank_mask:0xf\n\t" \
    "v_mul_f32_dpp v98, v101, v113 quad_perm:[0,0,2,2] row_mask:0xf bank_mask:0xf\n\t" \
    "v_mul_f32_dpp v99, v101, v113 quad_perm:[1,1,3,3] row_mask:0xf bank_mask:0xf\n\t" \
    "v_lshlrev_b32 v92, 16, v93\n\t" \
    "v_and_b32 v93, 0xffff0000, v93\n\t" \
    "v_lshlrev_b32 v94, 16, v95\n\t" \
    "v_and_b32 v95, 0xffff0000, v95\n\t" \
    "v_pk_fma_f32 v[92:93], v[92:93], v[98:99], s[78:79] op_sel:[0,0,0] op_sel_hi:[1,0,1]\n\t" \
    "v_pk_fma_f32 v[94:95], v[94:95], v[98:99], s[78:79] op_sel:[0,1,0] op_sel_hi:[1,1,1]\n\t" \
    "v_add_u32_dpp v96, v100, v112 quad_perm:[0,0,2,2] row_mask:0xf bank_mask:0xf\n\t" \
    "v_add_u32_dpp v97, v100, v112 quad_perm:[1,1,3,3] row_mask:0xf bank_mask:0xf\n\t" \
    "v_add_u32 v93, s84, v93\n\t" \
    "v_add_u32 v95, s84, v95\n\t" \
    "v_cmpx_ne_u32 vcc, 0, v98\n\t" \
    "ds_add_u64 v96, v[92:93]\n\t" \
    "s_mov_b64 exec, -1\n\t" \
    "v_cmpx_ne_u32 vcc, 0, v99\n\t" \
    "ds_add_u64 v97, v[94:95]\n\t" \
    "s_mov_b64 exec, -1\n\t" \
    "buffer_load_dwordx4 v[6:9], v104, %4, s94 offen\n\t" \
    "s_waitcnt vmcnt(1)\n\t" \
    "v_bfe_i32 v100, v25, 5, 1\n\t" \
    "v_bfi_b32 v101, v100, s88, v114\n\t" \
    "v_perm_b32 v101, v26, v24, v101\n\t" \
    "v_bfi_b32 v24, v100, v24, v26\n\t" \
    "v_perm_b32 v100, v25, v24, s89\n\t" \
    "v_lshlrev_b32 v100, 3, v100\n\t" \
    "v_bfi_b32 v26, s90, v100, v101\n\t" \
    "s_setprio 2\n\t" \
    "ds_write_b128 v104, v[14:17] offset:32768\n\t" \
    "s_mov_b64 exec, s[80:81]\n\t" \
    "ds_write_b64 v105, v[18:19] offset:49152\n\t" \
    "s_mov_b64 exec, -1\n\t" \
    "s_add_u32 s87, s94, 16384\n\t" \
    "buffer_load_dwordx4 v[10:13], v104, %4, s87 offen\n\t" \
    "s_setprio 1\n\t" \
    "v_ashrrev_i32 v101, 3, v32\n\t" \
    "v_lshrrev_b32 v100, 2, v32\n\t" \
    "v_and_b32 v101, 0x87ff0000, v101\n\t" \
    "s_mov_b64 vcc, s[82:83]\n\t" \
    "v_cndmask_b32_dpp v93, v73, v72, vcc quad_perm:[1,0,3,2] row_mask:0xf bank_mask:0xf\n\t" \
    "s_not_b64 vcc, vcc\n\t" \
    "v_and_b32 v100, 0x1fff0, v100\n\t" \
    "v_cndmask_b32_dpp v95, v72, v73, vcc quad_perm:[1,0,3,2] row_mask:0xf bank_mask:0xf\n\t" \
    "v_mul_f32_dpp v98, v101, v113 quad_perm:[0,0,2,2] row_mask:0xf bank_mask:0xf\n\t" \
    "v_mul_f32_dpp v99, v101, v113 quad_perm:[1,1,3,3] row_mask:0xf bank_mask:0xf\n\t" \
    "v_lshlrev_b32 v92, 16, v93\n\t" \
    "v_and_b32 v93, 0xffff0000, v93\n\t" \
    "v_lshlrev_b32 v94, 16, v95\n\t" \
    "v_and_b32 v95, 0xffff0000, v95\n\t" \
    "v_pk_fma_f32 v[92:93], v[92:93], v[98:99], s[78:79] op_sel:[0,0,0] op_sel_hi:[1,0,1]\n\t" \
    "v_pk_fma_f32 v[94:95], v[94:95], v[98:99], s[78:79] op_sel:[0,1,0] op_sel_hi:[1,1,1]\n\t" \
    "v_add_u32_dpp v96, v100, v112 quad_perm:[0,0,2,2] row_mask:0xf bank_mask:0xf\n\t" \
    "v_add_u32_dpp v97, v100, v112 quad_perm:[1,1,3,3] row_mask:0xf bank_mask:0xf\n\t" \
    "v_add_u32 v93, s84, v93\n\t" \
    "v_add_u32 v95, s84, v95\n\t" \
    "v_cmpx_ne_u32 vcc, 0, v98\n\t" \
    "ds_add_u64 v96, v[92:93]\n\t" \
    "s_mov_b64 exec, -1\n\t" \
    "v_cmpx_ne_u32 vcc, 0, v99\n\t" \
    "ds_add_u64 v97, v[94:95]\n\t" \
    "s_mov_b64 exec, -1\n\t" \
    "s_add_u32 s87, s94, 32768\n\t" \
    "buffer_load_dwordx4 v[14:17], v104, %4, s87 offen\n\t" \
    "s_waitcnt lgkmcnt(2)\n\t" \
    "s_barrier\n\t" \
    "s_setprio 3\n\t" \
    "s_mov_b64 exec, s[48:49]\n\t" \
    "ds_read_b64 v[60:61], v23 offset:0\n\t" \
    "s_mov_b64 exec, s[50:51]\n\t" \
    "ds_read_b64 v[62:63], v27 offset:0\n\t" \
    "s_mov_b64 exec, s[52:53]\n\t" \
    "ds_read_b64 v[80:81], v31 offset:0\n\t" \
    "s_mov_b64 exec, s[54:55]\n\t" \
    "ds_read_b64 v[82:83], v35 offset:0\n\t" \
    "s_mov_b64 exec, -1\n\t" \
    "s_add_u32 s87, s94, 49152\n\t" \
    "buffer_load_dwordx2 v[18:19], v105, %4, s87 offen\n\t" \
    "s_add_u32 s94, s94, 57152\n\t" \
    "v_add_u32 v30, v30, v103\n\t" \
    "v_and_b32 v30, -4, v30\n\t" \
    "buffer_load_dwordx3 v[28:30], v30, %5, 0 offen offset:1280 sc1\n\t" \
    "s_cmp_eq_u32 s92, 1\n\t" \
    "s_cbranch_scc1 26f\n\t" \
    "v_readlane_b32 s68, v108, 40\n\t" \
    "v_readlane_b32 s69, v108, 41\n\t" \
    "v_readlane_b32 s70, v108, 42\n\t" \
    "v_readlane_b32 s71, v108, 43\n\t" \
    "v_readlane_b32 s72, v108, 44\n\t" \
    "v_readlane_b32 s73, v108, 45\n\t" \
    "v_readlane_b32 s74, v108, 46\n\t" \
    "v_readlane_b32 s75, v108, 47\n\t" \
    "v_mbcnt_lo_u32_b32 v100, s68, 0\n\t" \
    "v_mbcnt_hi_u32_b32 v100, s69, v100\n\t" \
    "s_bcnt1_i32_b64 s87, s[68:69]\n\t" \
    "v_lshlrev_b32 v100, 2, v100\n\t" \
    "s_lshl2_add_u32 s86, s87, 0\n\t" \
    "ds_bpermute_b32 v42, v100, v108\n\t" \
    "v_mbcnt_lo_u32_b32 v101, s70, 0\n\t" \
    "v_mbcnt_hi_u32_b32 v101, s71, v101\n\t" \
    "s_bcnt1_i32_b64 s87, s[70:71]\n\t" \
    "v_lshl_add_u32 v101, v101, 2, s86\n\t" \
    "s_lshl2_add_u32 s86, s87, s86\n\t" \
    "ds_bpermute_b32 v46, v101, v108\n\t" \
    "v_mbcnt_lo_u32_b32 v100, s72, 0\n\t" \
    "v_mbcnt_hi_u32_b32 v100, s73, v100\n\t" \
    "s_bcnt1_i32_b64 s87, s[72:73]\n\t" \
    "v_lshl_add_u32 v100, v100, 2, s86\n\t" \
    "s_lshl2_add_u32 s86, s87, s86\n\t" \
    "ds_bpermute_b32 v50, v100, v108\n\t" \
    "v_mbcnt_lo_u32_b32 v101, s74, 0\n\t" \
    "v_mbcnt_hi_u32_b32 v101, s75, v101\n\t" \
    "s_bcnt1_i32_b64 s87, s[74:75]\n\t" \
    "v_lshl_add_u32 v101, v101, 2, s86\n\t" \
    "s_lshl2_add_u32 s86, s87, s86\n\t" \
    "ds_bpermute_b32 v54, v101, v108\n\t" \
    "s_bitcmp0_b32 s91, 2\n\t" \
    "s_cbranch_scc1 28f\n\t" \
    "v_readlane_b32 s76, v108, 48\n\t" \
    "v_readlane_b32 s77, v108, 49\n\t" \
    "s_nop 0\n\t" \
    "v_mbcnt_lo_u32_b32 v100, s76, 0\n\t" \
    "v_mbcnt_hi_u32_b32 v100, s77, v100\n\t" \
    "v_lshl_add_u32 v100, v100, 2, s86\n\t" \
    "ds_bpermute_b32 v58, v100, v108\n\t" \
    "28:\n\t" \
    "v_mov_b32 v108, v110\n\t" \
    "s_branch 27f\n\t" \
    "26:\n\t" \
    "v_bfrev_b32 v42, 1\n\t" \
    "v_bfrev_b32 v46, 1\n\t" \
    "v_bfrev_b32 v50, 1\n\t" \
    "v_bfrev_b32 v54, 1\n\t" \
    "27:\n\t" \
    "s_setprio 2\n\t" \
    "s_mov_b64 exec, s[56:57]\n\t" \
    "ds_read_b64 v[84:85], v39 offset:0\n\t" \
    "s_mov_b64 exec, s[58:59]\n\t" \
    "ds_read_b64 v[86:87], v43 offset:0\n\t" \
    "s_mov_b64 exec, s[60:61]\n\t" \
    "ds_read_b64 v[88:89], v47 offset:0\n\t" \
    "s_mov_b64 exec, s[62:63]\n\t" \
    "ds_read_b64 v[90:91], v51 offset:0\n\t" \
    "s_bitcmp0_b32 s91, 0\n\t" \
    "s_cbranch_scc1 29f\n\t" \
    "s_mov_b64 exec, s[64:65]\n\t" \
    "ds_read_b64 v[76:77], v55 offset:0\n\t" \
    "s_mov_b64 exec, s[66:67]\n\t" \
    "ds_read_b64 v[78:79], v59 offset:0\n\t" \
    "29:\n\t" \
    "s_mov_b64 exec, -1\n\t" \
    "s_setprio 1\n\t" \
    "v_ashrrev_i32 v101, 3, v33\n\t" \
    "v_lshrrev_b32 v100, 2, v33\n\t" \
    "v_and_b32 v101, 0x87ff0000, v101\n\t" \
    "s_mov_b64 vcc, s[82:83]\n\t" \
    "v_cndmask_b32_dpp v93, v75, v74, vcc quad_perm:[1,0,3,2] row_mask:0xf bank_mask:0xf\n\t" \
    "s_not_b64 vcc, vcc\n\t" \
    "v_and_b32 v100, 0x1fff0, v100\n\t" \
    "v_cndmask_b32_dpp v95, v74, v75, vcc quad_perm:[1,0,3,2] row_mask:0xf bank_mask:0xf\n\t" \
    "v_mul_f32_dpp v98, v101, v113 quad_perm:[0,0,2,2] row_mask:0xf bank_mask:0xf\n\t" \
    "v_mul_f32_dpp v99, v101, v113 quad_perm:[1,1,3,3] row_mask:0xf bank_mask:0xf\n\t" \
    "v_lshlrev_b32 v92, 16, v93\n\t" \
    "v_and_b32 v93, 0xffff0000, v93\n\t" \
    "v_lshlrev_b32 v94, 16, v95\n\t" \
    "v_and_b32 v95, 0xffff0000, v95\n\t" \
    "v_pk_fma_f32 v[92:93], v[92:93], v[98:99], s[78:79] op_sel:[0,0,0] op_sel_hi:[1,0,1]\n\t" \
    "v_pk_fma_f32 v[94:95], v[94:95], v[98:99], s[78:79] op_sel:[0,1,0] op_sel_hi:[1,1,1]\n\t" \
    "v_add_u32_dpp v96, v100, v112 quad_perm:[0,0,2,2] row_mask:0xf bank_mask:0xf\n\t" \
    "v_add_u32_dpp v97, v100, v112 quad_perm:[1,1,3,3] row_mask:0xf bank_mask:0xf\n\t" \
    "v_add_u32 v93, s84, v93\n\t" \
    "v_add_u32 v95, s84, v95\n\t" \
    "v_cmpx_ne_u32 vcc, 0, v98\n\t" \
    "ds_add_u64 v96, v[92:93]\n\t" \
    "s_mov_b64 exec, -1\n\t" \
    "v_cmpx_ne_u32 vcc, 0, v99\n\t" \
    "ds_add_u64 v97, v[94:95]\n\t" \
    "s_mov_b64 exec, -1\n\t" \
    "v_add_u32 v34, v34, v103\n\t" \
    "v_and_b32 v34, -4, v34\n\t" \
    "buffer_load_dwordx3 v[32:34], v34, %5, 0 offen offset:1920 sc1\n\t" \
    "s_bitcmp0_b32 s91, 1\n\t" \
    "s_cbranch_scc1 30f\n\t" \
    "v_add_u32 v38, v38, v103\n\t" \
    "v_and_b32 v38, -4, v38\n\t" \
    "buffer_load_dwordx3 v[36:38], v38, %5, 0 offen offset:2560 sc1\n\t" \
    "30:\n\t" \
    "s_waitcnt lgkmcnt(2)\n\t" \
    "s_barrier\n\t" \
    "s_setprio 3\n\t" \
    "s_waitcnt vmcnt(5)\n\t" \
    "ds_write_b128 v104, v[6:9] offset:0\n\t" \
    "s_waitcnt vmcnt(1)\n\t" \
    "v_bfe_i32 v100, v29, 5, 1\n\t" \
    "v_bfi_b32 v101, v100, s88, v114\n\t" \
    "v_perm_b32 v101, v30, v28, v101\n\t" \
    "v_bfi_b32 v28, v100, v28, v30\n\t" \
    "v_perm_b32 v100, v29, v28, s89\n\t" \
    "v_lshlrev_b32 v100, 3, v100\n\t" \
    "v_bfi_b32 v30, s90, v100, v101\n\t" \
    "v_ashrrev_i32 v101, 3, v40\n\t" \
    "v_lshrrev_b32 v100, 2, v40\n\t" \
    "v_and_b32 v101, 0x87ff0000, v101\n\t" \
    "s_mov_b64 vcc, s[82:83]\n\t" \
    "v_cndmask_b32_dpp v93, v61, v60, vcc quad_perm:[1,0,3,2] row_mask:0xf bank_mask:0xf\n\t" \
    "s_not_b64 vcc, vcc\n\t" \
    "v_and_b32 v100, 0x1fff0, v100\n\t" \
    "v_cndmask_b32_dpp v95, v60, v61, vcc quad_perm:[1,0,3,2] row_mask:0xf bank_mask:0xf\n\t" \
    "v_mul_f32_dpp v98, v101, v113 quad_perm:[0,0,2,2] row_mask:0xf bank_mask:0xf\n\t" \
    "v_mul_f32_dpp v99, v101, v113 quad_perm:[1,1,3,3] row_mask:0xf bank_mask:0xf\n\t" \
    "v_lshlrev_b32 v92, 16, v93\n\t" \
    "v_and_b32 v93, 0xffff0000, v93\n\t" \
    "v_lshlrev_b32 v94, 16, v95\n\t" \
    "v_and_b32 v95, 0xffff0000, v95\n\t" \
    "v_pk_fma_f32 v[92:93], v[92:93], v[98:99], s[78:79] op_sel:[0,0,0] op_sel_hi:[1,0,1]\n\t" \
    "v_pk_fma_f32 v[94:95], v[94:95], v[98:99], s[78:79] op_sel:[0,1,0] op_sel_hi:[1,1,1]\n\t" \
    "v_add_u32_dpp v96, v100, v112 quad_perm:[0,0,2,2] row_mask:0xf bank_mask:0xf\n\t" \
    "v_add_u32_dpp v97, v100, v112 quad_perm:[1,1,3,3] row_mask:0xf bank_mask:0xf\n\t" \
    "v_add_u32 v93, s84, v93\n\t" \
    "v_add_u32 v95, s84, v95\n\t" \
    "ds_add_u64 v96, v[92:93]\n\t" \
    "ds_add_u64 v97, v[94:95]\n\t" \
    "buffer_load_dwordx4 v[6:9], v104, %4, s94 offen\n\t" \
    "ds_write_b128 v104, v[10:13] offset:16384\n\t" \
    "s_setprio 2\n\t" \
    "s_add_u32 s87, s94, 16384\n\t" \
    "buffer_load_dwordx4 v[10:13], v104, %4, s87 offen\n\t" \
    "s_bitcmp0_b32 s91, 0\n\t" \
    "s_cbranch_scc1 31f\n\t" \
    "v_ashrrev_i32 v101, 3, v56\n\t" \
    "v_lshrrev_b32 v100, 2, v56\n\t" \
    "v_and_b32 v101, 0x87ff0000, v101\n\t" \
    "s_mov_b64 vcc, s[82:83]\n\t" \
    "v_cndmask_b32_dpp v93, v77, v76, vcc quad_perm:[1,0,3,2] row_mask:0xf bank_mask:0xf\n\t" \
    "s_not_b64 vcc, vcc\n\t" \
    "v_and_b32 v100, 0x1fff0, v100\n\t" \
    "v_cndmask_b32_dpp v95, v76, v77, vcc quad_perm:[1,0,3,2] row_mask:0xf bank_mask:0xf\n\t" \
    "v_mul_f32_dpp v98, v101, v113 quad_perm:[0,0,2,2] row_mask:0xf bank_mask:0xf\n\t" \
    "v_mul_f32_dpp v99, v101, v113 quad_perm:[1,1,3,3] row_mask:0xf bank_mask:0xf\n\t" \
    "v_lshlrev_b32 v92, 16, v93\n\t" \
    "v_and_b32 v93, 0xffff0000, v93\n\t" \
    "v_lshlrev_b32 v94, 16, v95\n\t" \
    "v_and_b32 v95, 0xffff0000, v95\n\t" \
    "v_pk_fma_f32 v[92:93], v[92:93], v[98:99], s[78:79] op_sel:[0,0,0] op_sel_hi:[1,0,1]\n\t" \
    "v_pk_fma_f32 v[94:95], v[94:95], v[98:99], s[78:79] op_sel:[0,1,0] op_sel_hi:[1,1,1]\n\t" \
    "v_add_u32_dpp v96, v100, v112 quad_perm:[0,0,2,2] row_mask:0xf bank_mask:0xf\n\t" \
    "v_add_u32_dpp v97, v100, v112 quad_perm:[1,1,3,3] row_mask:0xf bank_mask:0xf\n\t" \
    "v_add_u32 v93, s84, v93\n\t" \
    "v_add_u32 v95, s84, v95\n\t" \
    "v_cmpx_ne_u32 vcc, 0, v98\n\t" \
    "ds_add_u64 v96, v[92:93]\n\t" \
    "s_mov_b64 exec, -1\n\t" \
    "v_cmpx_ne_u32 vcc, 0, v99\n\t" \
    "ds_add_u64 v97, v[94:95]\n\t" \
    "s_mov_b64 exec, -1\n\t" \
    "v_ashrrev_i32 v101, 3, v57\n\t" \
    "v_lshrrev_b32 v100, 2, v57\n\t" \
    "v_and_b32 v101, 0x87ff0000, v101\n\t" \
    "s_mov_b64 vcc, s[82:83]\n\t" \
    "v_cndmask_b32_dpp v93, v79, v78, vcc quad_perm:[1,0,3,2] row_mask:0xf bank_mask:0xf\n\t" \
    "s_not_b64 vcc, vcc\n\t" \
    "v_and_b32 v100, 0x1fff0, v100\n\t" \
    "v_cndmask_b32_dpp v95, v78, v79, vcc quad_perm:[1,0,3,2] row_mask:0xf bank_mask:0xf\n\t" \
    "v_mul_f32_dpp v98, v101, v113 quad_perm:[0,0,2,2] row_mask:0xf bank_mask:0xf\n\t" \
    "v_mul_f32_dpp v99, v101, v113 quad_perm:[1,1,3,3] row_mask:0xf bank_mask:0xf\n\t" \
    "v_lshlrev_b32 v92, 16, v93\n\t" \
    "v_and_b32 v93, 0xffff0000, v93\n\t" \
    "v_lshlrev_b32 v94, 16, v95\n\t" \
    "v_and_b32 v95, 0xffff0000, v95\n\t" \
    "v_pk_fma_f32 v[92:93], v[92:93], v[98:99], s[78:79] op_sel:[0,0,0] op_sel_hi:[1,0,1]\n\t" \
    "v_pk_fma_f32 v[94:95], v[94:95], v[98:99], s[78:79] op_sel:[0,1,0] op_sel_hi:[1,1,1]\n\t" \
    "v_add_u32_dpp v96, v100, v112 quad_perm:[0,0,2,2] row_mask:0xf bank_mask:0xf\n\t" \
    "v_add_u32_dpp v97, v100, v112 quad_perm:[1,1,3,3] row_mask:0xf bank_mask:0xf\n\t" \
    "v_add_u32 v93, s84, v93\n\t" \
    "v_add_u32 v95, s84, v95\n\t" \
    "v_cmpx_ne_u32 vcc, 0, v98\n\t" \
    "ds_add_u64 v96, v[92:93]\n\t" \
    "s_mov_b64 exec, -1\n\t" \
    "v_cmpx_ne_u32 vcc, 0, v99\n\t" \
    "ds_add_u64 v97, v[94:95]\n\t" \
    "s_mov_b64 exec, -1\n\t" \
    "31:\n\t" \
    "s_waitcnt vmcnt(2)\n\t" \
    "v_bfe_i32 v100, v33, 5, 1\n\t" \
    "v_bfi_b32 v101, v100, s88, v114\n\t" \
    "v_perm_b32 v101, v34, v32, v101\n\t" \
    "v_bfi_b32 v32, v100, v32, v34\n\t" \
    "v_perm_b32 v100, v33, v32, s89\n\t" \
    "v_lshlrev_b32 v100, 3, v100\n\t" \
    "v_bfi_b32 v34, s90, v100, v101\n\t" \
    "v_ashrrev_i32 v101, 3, v41\n\t" \
    "v_lshrrev_b32 v100, 2, v41\n\t" \
    "v_and_b32 v101, 0x87ff0000, v101\n\t" \
    "s_mov_b64 vcc, s[82:83]\n\t" \
    "v_cndmask_b32_dpp v93, v63, v62, vcc quad_perm:[1,0,3,2] row_mask:0xf bank_mask:0xf\n\t" \
    "s_not_b64 vcc, vcc\n\t" \
    "v_and_b32 v100, 0x1fff0, v100\n\t" \
    "v_cndmask_b32_dpp v95, v62, v63, vcc quad_perm:[1,0,3,2] row_mask:0xf bank_mask:0xf\n\t" \
    "v_mul_f32_dpp v98, v101, v113 quad_perm:[0,0,2,2] row_mask:0xf bank_mask:0xf\n\t" \
    "v_mul_f32_dpp v99, v101, v113 quad_perm:[1,1,3,3] row_mask:0xf bank_mask:0xf\n\t" \
    "v_lshlrev_b32 v92, 16, v93\n\t" \
    "v_and_b32 v93, 0xffff0000, v93\n\t" \
    "v_lshlrev_b32 v94, 16, v95\n\t" \
    "v_and_b32 v95, 0xffff0000, v95\n\t" \
    "v_pk_fma_f32 v[92:93], v[92:93], v[98:99], s[78:79] op_sel:[0,0,0] op_sel_hi:[1,0,1]\n\t" \
    "v_pk_fma_f32 v[94:95], v[94:95], v[98:99], s[78:79] op_sel:[0,1,0] op_sel_hi:[1,1,1]\n\t" \
    "v_add_u32_dpp v96, v100, v112 quad_perm:[0,0,2,2] row_mask:0xf bank_mask:0xf\n\t" \
    "v_add_u32_dpp v97, v100, v112 quad_perm:[1,1,3,3] row_mask:0xf bank_mask:0xf\n\t" \
    "v_add_u32 v93, s84, v93\n\t" \
    "v_add_u32 v95, s84, v95\n\t" \
    "ds_add_u64 v96, v[92:93]\n\t" \
    "ds_add_u64 v97, v[94:95]\n\t" \
    "ds_write_b128 v104, v[14:17] offset:32768\n\t" \
    "s_setprio 1\n\t" \
    "s_add_u32 s87, s94, 32768\n\t" \
    "buffer_load_dwordx4 v[14:17], v104, %4, s87 offen\n\t" \
    "v_and_b32 v23, 0xffff, v22\n\t" \
    "v_lshrrev_b32 v27, 16, v22\n\t" \
    "v_cmp_nlt_f32 s[48:49], |v20|, 2.0\n\t" \
    "v_cmp_nlt_f32 s[50:51], |v21|, 2.0\n\t" \
    "v_and_b32 v31, 0xffff, v26\n\t" \
    "v_lshrrev_b32 v35, 16, v26\n\t" \
    "v_cmp_nlt_f32 s[52:53], |v24|, 2.0\n\t" \
    "v_cmp_nlt_f32 s[54:55], |v25|, 2.0\n\t" \
    "v_and_b32 v39, 0xffff, v30\n\t" \
    "v_lshrrev_b32 v43, 16, v30\n\t" \
    "v_cmp_nlt_f32 s[56:57], |v28|, 2.0\n\t" \
    "v_cmp_nlt_f32 s[58:59], |v29|, 2.0\n\t" \
    "v_and_b32 v47, 0xffff, v34\n\t" \
    "v_lshrrev_b32 v51, 16, v34\n\t" \
    "v_cmp_nlt_f32 s[60:61], |v32|, 2.0\n\t" \
    "v_cmp_nlt_f32 s[62:63], |v33|, 2.0\n\t" \
    "s_bitcmp0_b32 s91, 1\n\t" \
    "s_cbranch_scc1 32f\n\t" \
    "s_waitcnt vmcnt(3)\n\t" \
    "v_bfe_i32 v100, v37, 5, 1\n\t" \
    "v_bfi_b32 v101, v100, s88, v114\n\t" \
    "v_perm_b32 v101, v38, v36, v101\n\t" \
    "v_bfi_b32 v36, v100, v36, v38\n\t" \
    "v_perm_b32 v100, v37, v36, s89\n\t" \
    "v_lshlrev_b32 v100, 3, v100\n\t" \
    "v_bfi_b32 v38, s90, v100, v101\n\t" \
    "v_and_b32 v55, 0xffff, v38\n\t" \
    "v_lshrrev_b32 v59, 16, v38\n\t" \
    "v_cmp_nlt_f32 s[64:65], |v36|, 2.0\n\t" \
    "v_cmp_nlt_f32 s[66:67], |v37|, 2.0\n\t" \
    "32:\n\t" \
    "v_add_u32 v42, v42, v103\n\t" \
    "v_and_b32 v42, -4, v42\n\t" \
    "buffer_load_dwordx3 v[40:42], v42, %5, 0 offen offset:0 sc1\n\t" \
    "s_setprio 0\n\t" \
    "v_ashrrev_i32 v101, 3, v44\n\t" \
    "v_lshrrev_b32 v100, 2, v44\n\t" \
    "v_and_b32 v101, 0x87ff0000, v101\n\t" \
    "s_mov_b64 vcc, s[82:83]\n\t" \
    "v_cndmask_b32_dpp v93, v81, v80, vcc quad_perm:[1,0,3,2] row_mask:0xf bank_mask:0xf\n\t" \
    "s_not_b64 vcc, vcc\n\t" \
    "v_and_b32 v100, 0x1fff0, v100\n\t" \
    "v_cndmask_b32_dpp v95, v80, v81, vcc quad_perm:[1,0,3,2] row_mask:0xf bank_mask:0xf\n\t" \
    "v_mul_f32_dpp v98, v101, v113 quad_perm:[0,0,2,2] row_mask:0xf bank_mask:0xf\n\t" \
    "v_mul_f32_dpp v99, v101, v113 quad_perm:[1,1,3,3] row_mask:0xf bank_mask:0xf\n\t" \
    "v_lshlrev_b32 v92, 16, v93\n\t" \
    "v_and_b32 v93, 0xffff0000, v93\n\t" \
    "v_lshlrev_b32 v94, 16, v95\n\t" \
    "v_and_b32 v95, 0xffff0000, v95\n\t" \
    "v_pk_fma_f32 v[92:93], v[92:93], v[98:99], s[78:79] op_sel:[0,0,0] op_sel_hi:[1,0,1]\n\t" \
    "v_pk_fma_f32 v[94:95], v[94:95], v[98:99], s[78:79] op_sel:[0,1,0] op_sel_hi:[1,1,1]\n\t" \
    "v_add_u32_dpp v96, v100, v112 quad_perm:[0,0,2,2] row_mask:0xf bank_mask:0xf\n\t" \
    "v_add_u32_dpp v97, v100, v112 quad_perm:[1,1,3,3] row_mask:0xf bank_mask:0xf\n\t" \
    "v_add_u32 v93, s84, v93\n\t" \
    "v_add_u32 v95, s84, v95\n\t" \
    "ds_add_u64 v96, v[92:93]\n\t" \
    "ds_add_u64 v97, v[94:95]\n\t" \
    "s_mov_b64 exec, s[80:81]\n\t" \
    "ds_write_b64 v105, v[18:19] offset:49152\n\t" \
    "s_mov_b64 exec, -1\n\t" \
    "s_lshr_b32 s91, s91, 1\n\t" \
    "s_waitcnt lgkmcnt(0)\n\t" \
    "s_branch 33f\n\t" \
    ".p2align 12\n\t" \
    "kw%c14_3:\n\t" \
    "s_setpc_b64 vcc\n\t" \
    "33:\n\t" \
    "s_barrier\n\t" \
    "s_setprio 3\n\t" \
    "s_not_b64 exec, s[48:49]\n\t" \
    "ds_read_b64 v[60:61], v23 offset:0\n\t" \
    "s_not_b64 exec, s[50:51]\n\t" \
    "ds_read_b64 v[62:63], v27 offset:0\n\t" \
    "s_not_b64 exec, s[52:53]\n\t" \
    "ds_read_b64 v[64:65], v31 offset:0\n\t" \
    "s_not_b64 exec, s[54:55]\n\t" \
    "ds_read_b64 v[66:67], v35 offset:0\n\t" \
    "s_mov_b64 exec, -1\n\t" \
    "v_ashrrev_i32 v101, 3, v45\n\t" \
    "v_lshrrev_b32 v100, 2, v45\n\t" \
    "v_and_b32 v101, 0x87ff0000, v101\n\t" \
    "s_mov_b64 vcc, s[82:83]\n\t" \
    "v_cndmask_b32_dpp v93, v83, v82, vcc quad_perm:[1,0,3,2] row_mask:0xf bank_mask:0xf\n\t" \
    "s_not_b64 vcc, vcc\n\t" \
    "v_and_b32 v100, 0x1fff0, v100\n\t" \
    "v_cndmask_b32_dpp v95, v82, v83, vcc quad_perm:[1,0,3,2] row_mask:0xf bank_mask:0xf\n\t" \
    "v_mul_f32_dpp v98, v101, v113 quad_perm:[0,0,2,2] row_mask:0xf bank_mask:0xf\n\t" \
    "v_mul_f32_dpp v99, v101, v113 quad_perm:[1,1,3,3] row_mask:0xf bank_mask:0xf\n\t" \
    "v_lshlrev_b32 v92, 16, v93\n\t" \
    "v_and_b32 v93, 0xffff0000, v93\n\t" \
    "v_lshlrev_b32 v94, 16, v95\n\t" \
    "v_and_b32 v95, 0xffff0000, v95\n\t" \
    "v_pk_fma_f32 v[92:93], v[92:93], v[98:99], s[78:79] op_sel:[0,0,0] op_sel_hi:[1,0,1]\n\t" \
    "v_pk_fma_f32 v[94:95], v[94:95], v[98:99], s[78:79] op_sel:[0,1,0] op_sel_hi:[1,1,1]\n\t" \
    "v_add_u32_dpp v96, v100, v112 quad_perm:[0,0,2,2] row_mask:0xf bank_mask:0xf\n\t" \
    "v_add_u32_dpp v97, v100, v112 quad_perm:[1,1,3,3] row_mask:0xf bank_mask:0xf\n\t" \
    "v_add_u32 v93, s84, v93\n\t" \
    "v_add_u32 v95, s84, v95\n\t" \
    "v_cmpx_ne_u32 vcc, 0, v98\n\t" \
    "ds_add_u64 v96, v[92:93]\n\t" \
    "s_mov_b64 exec, -1\n\t" \
    "v_cmpx_ne_u32 vcc, 0, v99\n\t" \
    "ds_add_u64 v97, v[94:95]\n\t" \
    "s_mov_b64 exec, -1\n\t" \
    "s_setprio 2\n\t" \
    "s_not_b64 exec, s[56:57]\n\t" \
    "ds_read_b64 v[68:69], v39 offset:0\n\t" \
    "s_not_b64 exec, s[58:59]\n\t" \
    "ds_read_b64 v[70:71], v43 offset:0\n\t" \
    "s_not_b64 exec, s[60:61]\n\t" \
    "ds_read_b64 v[72:73], v47 offset:0\n\t" \
    "s_not_b64 exec, s[62:63]\n\t" \
    "ds_read_b64 v[74:75], v51 offset:0\n\t" \
    "s_bitcmp0_b32 s91, 0\n\t" \
    "s_cbranch_scc1 34f\n\t" \
    "s_not_b64 exec, s[64:65]\n\t" \
    "ds_read_b64 v[76:77], v55 offset:0\n\t" \
    "s_not_b64 exec, s[66:67]\n\t" \
    "ds_read_b64 v[78:79], v59 offset:0\n\t" \
    "34:\n\t" \
    "s_mov_b64 exec, -1\n\t" \
    "s_add_u32 s87, s94, 49152\n\t" \
    "buffer_load_dwordx2 v[18:19], v105, %4, s87 offen\n\t" \
    "s_add_u32 s94, s94, 57152\n\t" \
    "s_cmp_eq_u32 s92, 1\n\t" \
    "s_cselect_b32 s94, 0x80000000, s94\n\t" \
    "v_add_u32 v46, v46, v103\n\t" \
    "v_and_b32 v46, -4, v46\n\t" \
    "buffer_load_dwordx3 v[44:46], v46, %5, 0 offen offset:640 sc1\n\t" \
    "s_waitcnt vmcnt(2)\n\t" \
    "v_bfe_i32 v100, v41, 5, 1\n\t" \
    "v_bfi_b32 v101, v100, s88, v114\n\t" \
    "v_perm_b32 v101, v42, v40, v101\n\t" \
    "v_bfi_b32 v40, v100, v40, v42\n\t" \
    "v_perm_b32 v100, v41, v40, s89\n\t" \
    "v_lshlrev_b32 v100, 3, v100\n\t" \
    "v_bfi_b32 v42, s90, v100, v101\n\t" \
    "s_setprio 1\n\t" \
    "v_ashrrev_i32 v101, 3, v48\n\t" \
    "v_lshrrev_b32 v100, 2, v48\n\t" \
    "v_and_b32 v101, 0x87ff0000, v101\n\t" \
    "s_mov_b64 vcc, s[82:83]\n\t" \
    "v_cndmask_b32_dpp v93, v85, v84, vcc quad_perm:[1,0,3,2] row_mask:0xf bank_mask:0xf\n\t" \
    "s_not_b64 vcc, vcc\n\t" \
    "v_and_b32 v100, 0x1fff0, v100\n\t" \
    "v_cndmask_b32_dpp v95, v84, v85, vcc quad_perm:[1,0,3,2] row_mask:0xf bank_mask:0xf\n\t" \
    "v_mul_f32_dpp v98, v101, v113 quad_perm:[0,0,2,2] row_mask:0xf bank_mask:0xf\n\t" \
    "v_mul_f32_dpp v99, v101, v113 quad_perm:[1,1,3,3] row_mask:0xf bank_mask:0xf\n\t" \
    "v_lshlrev_b32 v92, 16, v93\n\t" \
    "v_and_b32 v93, 0xffff0000, v93\n\t" \
    "v_lshlrev_b32 v94, 16, v95\n\t" \
    "v_and_b32 v95, 0xffff0000, v95\n\t" \
    "v_pk_fma_f32 v[92:93], v[92:93], v[98:99], s[78:79] op_sel:[0,0,0] op_sel_hi:[1,0,1]\n\t" \
    "v_pk_fma_f32 v[94:95], v[94:95], v[98:99], s[78:79] op_sel:[0,1,0] op_sel_hi:[1,1,1]\n\t" \
    "v_add_u32_dpp v96, v100, v112 quad_perm:[0,0,2,2] row_mask:0xf bank_mask:0xf\n\t" \
    "v_add_u32_dpp v97, v100, v112 quad_perm:[1,1,3,3] row_mask:0xf bank_mask:0xf\n\t" \
    "v_add_u32 v93, s84, v93\n\t" \
    "v_add_u32 v95, s84, v95\n\t" \
    "v_cmpx_ne_u32 vcc, 0, v98\n\t" \
    "ds_add_u64 v96, v[92:93]\n\t" \
    "s_mov_b64 exec, -1\n\t" \
    "v_cmpx_ne_u32 vcc, 0, v99\n\t" \
    "ds_add_u64 v97, v[94:95]\n\t" \
    "s_mov_b64 exec, -1\n\t" \
    "s_waitcnt lgkmcnt(2)\n\t" \
    "s_barrier\n\t" \
    "s_setprio 3\n\t" \
    "ds_write_b128 v104, v[6:9] offset:0\n\t" \
    "ds_write_b128 v104, v[10:13] offset:16384\n\t" \
    "v_ashrrev_i32 v101, 3, v49\n\t" \
    "v_lshrrev_b32 v100, 2, v49\n\t" \
    "v_and_b32 v101, 0x87ff0000, v101\n\t" \
    "s_mov_b64 vcc, s[82:83]\n\t" \
    "v_cndmask_b32_dpp v93, v87, v86, vcc quad_perm:[1,0,3,2] row_mask:0xf bank_mask:0xf\n\t" \
    "s_not_b64 vcc, vcc\n\t" \
    "v_and_b32 v100, 0x1fff0, v100\n\t" \
    "v_cndmask_b32_dpp v95, v86, v87, vcc quad_perm:[1,0,3,2] row_mask:0xf bank_mask:0xf\n\t" \
    "v_mul_f32_dpp v98, v101, v113 quad_perm:[0,0,2,2] row_mask:0xf bank_mask:0xf\n\t" \
    "v_mul_f32_dpp v99, v101, v113 quad_perm:[1,1,3,3] row_mask:0xf bank_mask:0xf\n\t" \
    "v_lshlrev_b32 v92, 16, v93\n\t" \
    "v_and_b32 v93, 0xffff0000, v93\n\t" \
    "v_lshlrev_b32 v94, 16, v95\n\t" \
    "v_and_b32 v95, 0xffff0000, v95\n\t" \
    "v_pk_fma_f32 v[92:93], v[92:93], v[98:99], s[78:79] op_sel:[0,0,0] op_sel_hi:[1,0,1]\n\t" \
    "v_pk_fma_f32 v[94:95], v[94:95], v[98:99], s[78:79] op_sel:[0,1,0] op_sel_hi:[1,1,1]\n\t" \
    "v_add_u32_dpp v96, v100, v112 quad_perm:[0,0,2,2] row_mask:0xf bank_mask:0xf\n\t" \
    "v_add_u32_dpp v97, v100, v112 quad_perm:[1,1,3,3] row_mask:0xf bank_mask:0xf\n\t" \
    "v_add_u32 v93, s84, v93\n\t" \
    "v_add_u32 v95, s84, v95\n\t" \
    "v_cmpx_ne_u32 vcc, 0, v98\n\t" \
    "ds_add_u64 v96, v[92:93]\n\t" \
    "s_mov_b64 exec, -1\n\t" \
    "v_cmpx_ne_u32 vcc, 0, v99\n\t" \
    "ds_add_u64 v97, v[94:95]\n\t" \
    "s_mov_b64 exec, -1\n\t" \
    "buffer_load_dwordx4 v[6:9], v104, %4, s94 offen\n\t" \
    "s_waitcnt vmcnt(1)\n\t" \
    "v_bfe_i32 v100, v45, 5, 1\n\t" \
    "v_bfi_b32 v101, v100, s88, v114\n\t" \
    "v_perm_b32 v101, v46, v44, v101\n\t" \
    "v_bfi_b32 v44, v100, v44, v46\n\t" \
    "v_perm_b32 v100, v45, v44, s89\n\t" \
    "v_lshlrev_b32 v100, 3, v100\n\t" \
    "v_bfi_b32 v46, s90, v100, v101\n\t" \
    "s_setprio 2\n\t" \
    "ds_write_b128 v104, v[14:17] offset:32768\n\t" \
    "s_mov_b64 exec, s[80:81]\n\t" \
    "ds_write_b64 v105, v[18:19] offset:49152\n\t" \
    "s_mov_b64 exec, -1\n\t" \
    "s_add_u32 s87, s94, 16384\n\t" \
    "buffer_load_dwordx4 v[10:13], v104, %4, s87 offen\n\t" \
    "s_setprio 1\n\t" \
    "v_ashrrev_i32 v101, 3, v52\n\t" \
    "v_lshrrev_b32 v100, 2, v52\n\t" \
    "v_and_b32 v101, 0x87ff0000, v101\n\t" \
    "s_mov_b64 vcc, s[82:83]\n\t" \
    "v_cndmask_b32_dpp v93, v89, v88, vcc quad_perm:[1,0,3,2] row_mask:0xf bank_mask:0xf\n\t" \
    "s_not_b64 vcc, vcc\n\t" \
    "v_and_b32 v100, 0x1fff0, v100\n\t" \
    "v_cndmask_b32_dpp v95, v88, v89, vcc quad_perm:[1,0,3,2] row_mask:0xf bank_mask:0xf\n\t" \
    "v_mul_f32_dpp v98, v101, v113 quad_perm:[0,0,2,2] row_mask:0xf bank_mask:0xf\n\t" \
    "v_mul_f32_dpp v99, v101, v113 quad_perm:[1,1,3,3] row_mask:0xf bank_mask:0xf\n\t" \
    "v_lshlrev_b32 v92, 16, v93\n\t" \
    "v_and_b32 v93, 0xffff0000, v93\n\t" \
    "v_lshlrev_b32 v94, 16, v95\n\t" \
    "v_and_b32 v95, 0xffff0000, v95\n\t" \
    "v_pk_fma_f32 v[92:93], v[92:93], v[98:99], s[78:79] op_sel:[0,0,0] op_sel_hi:[1,0,1]\n\t" \
    "v_pk_fma_f32 v[94:95], v[94:95], v[98:99], s[78:79] op_sel:[0,1,0] op_sel_hi:[1,1,1]\n\t" \
    "v_add_u32_dpp v96, v100, v112 quad_perm:[0,0,2,2] row_mask:0xf bank_mask:0xf\n\t" \
    "v_add_u32_dpp v97, v100, v112 quad_perm:[1,1,3,3] row_mask:0xf bank_mask:0xf\n\t" \
    "v_add_u32 v93, s84, v93\n\t" \
    "v_add_u32 v95, s84, v95\n\t" \
    "v_cmpx_ne_u32 vcc, 0, v98\n\t" \
    "ds_add_u64 v96, v[92:93]\n\t" \
    "s_mov_b64 exec, -1\n\t" \
    "v_cmpx_ne_u32 vcc, 0, v99\n\t" \
    "ds_add_u64 v97, v[94:95]\n\t" \
    "s_mov_b64 exec, -1\n\t" \
    "s_add_u32 s87, s94, 32768\n\t" \
    "buffer_load_dwordx4 v[14:17], v104, %4, s87 offen\n\t" \
    "s_waitcnt lgkmcnt(2)\n\t" \
    "s_barrier\n\t" \
    "s_setprio 3\n\t" \
    "s_mov_b64 exec, s[48:49]\n\t" \
    "ds_read_b64 v[60:61], v23 offset:0\n\t" \
    "s_mov_b64 exec, s[50:51]\n\t" \
    "ds_read_b64 v[62:63], v27 offset:0\n\t" \
    "s_mov_b64 exec, s[52:53]\n\t" \
    "ds_read_b64 v[64:65], v31 offset:0\n\t" \
    "s_mov_b64 exec, s[54:55]\n\t" \
    "ds_read_b64 v[66:67], v35 offset:0\n\t" \
    "s_mov_b64 exec, -1\n\t" \
    "s_add_u32 s87, s94, 49152\n\t" \
    "buffer_load_dwordx2 v[18:19], v105, %4, s87 offen\n\t" \
    "s_add_u32 s94, s94, 57152\n\t" \
    "v_add_u32 v50, v50, v103\n\t" \
    "v_and_b32 v50, -4, v50\n\t" \
    "buffer_load_dwordx3 v[48:50], v50, %5, 0 offen offset:1280 sc1\n\t" \
    "s_cmp_eq_u32 s92, 1\n\t" \
    "s_cbranch_scc1 35f\n\t" \
    "v_readlane_b32 s68, v109, 40\n\t" \
    "v_readlane_b32 s69, v109, 41\n\t" \
    "v_readlane_b32 s70, v109, 42\n\t" \
    "v_readlane_b32 s71, v109, 43\n\t" \
    "v_readlane_b32 s72, v109, 44\n\t" \
    "v_readlane_b32 s73, v109, 45\n\t" \
    "v_readlane_b32 s74, v109, 46\n\t" \
    "v_readlane_b32 s75, v109, 47\n\t" \
    "v_mbcnt_lo_u32_b32 v100, s68, 0\n\t" \
    "v_mbcnt_hi_u32_b32 v100, s69, v100\n\t" \
    "s_bcnt1_i32_b64 s87, s[68:69]\n\t" \
    "v_lshlrev_b32 v100, 2, v100\n\t" \
    "s_lshl2_add_u32 s86, s87, 0\n\t" \
    "ds_bpermute_b32 v22, v100, v109\n\t" \
    "v_mbcnt_lo_u32_b32 v101, s70, 0\n\t" \
    "v_mbcnt_hi_u32_b32 v101, s71, v101\n\t" \
    "s_bcnt1_i32_b64 s87, s[70:71]\n\t" \
    "v_lshl_add_u32 v101, v101, 2, s86\n\t" \
    "s_lshl2_add_u32 s86, s87, s86\n\t" \
    "ds_bpermute_b32 v26, v101, v109\n\t" \
    "v_mbcnt_lo_u32_b32 v100, s72, 0\n\t" \
    "v_mbcnt_hi_u32_b32 v100, s73, v100\n\t" \
    "s_bcnt1_i32_b64 s87, s[72:73]\n\t" \
    "v_lshl_add_u32 v100, v100, 2, s86\n\t" \
    "s_lshl2_add_u32 s86, s87, s86\n\t" \
    "ds_bpermute_b32 v30, v100, v109\n\t" \
    "v_mbcnt_lo_u32_b32 v101, s74, 0\n\t" \
    "v_mbcnt_hi_u32_b32 v101, s75, v101\n\t" \
    "s_bcnt1_i32_b64 s87, s[74:75]\n\t" \
    "v_lshl_add_u32 v101, v101, 2, s86\n\t" \
    "s_lshl2_add_u32 s86, s87, s86\n\t" \
    "ds_bpermute_b32 v34, v101, v109\n\t" \
    "s_bitcmp0_b32 s91, 2\n\t" \
    "s_cbranch_scc1 37f\n\t" \
    "v_readlane_b32 s76, v109, 48\n\t" \
    "v_readlane_b32 s77, v109, 49\n\t" \
    "s_nop 0\n\t" \
    "v_mbcnt_lo_u32_b32 v100, s76, 0\n\t" \
    "v_mbcnt_hi_u32_b32 v100, s77, v100\n\t" \
    "v_lshl_add_u32 v100, v100, 2, s86\n\t" \
    "ds_bpermute_b32 v38, v100, v109\n\t" \
    "37:\n\t" \
    "v_mov_b32 v109, v111\n\t" \
    "35:\n\t" \
    "s_setprio 2\n\t" \
    "s_mov_b64 exec, s[56:57]\n\t" \
    "ds_read_b64 v[68:69], v39 offset:0\n\t" \
    "s_mov_b64 exec, s[58:59]\n\t" \
    "ds_read_b64 v[70:71], v43 offset:0\n\t" \
    "s_mov_b64 exec, s[60:61]\n\t" \
    "ds_read_b64 v[72:73], v47 offset:0\n\t" \
    "s_mov_b64 exec, s[62:63]\n\t" \
    "ds_read_b64 v[74:75], v51 offset:0\n\t" \
    "s_bitcmp0_b32 s91, 0\n\t" \
    "s_cbranch_scc1 38f\n\t" \
    "s_mov_b64 exec, s[64:65]\n\t" \
    "ds_read_b64 v[76:77], v55 offset:0\n\t" \
    "s_mov_b64 exec, s[66:67]\n\t" \
    "ds_read_b64 v[78:79], v59 offset:0\n\t" \
    "38:\n\t" \
    "s_mov_b64 exec, -1\n\t" \
    "s_setprio 1\n\t" \
    "v_ashrrev_i32 v101, 3, v53\n\t" \
    "v_lshrrev_b32 v100, 2, v53\n\t" \
    "v_and_b32 v101, 0x87ff0000, v101\n\t" \
    "s_mov_b64 vcc, s[82:83]\n\t" \
    "v_cndmask_b32_dpp v93, v91, v90, vcc quad_perm:[1,0,3,2] row_mask:0xf bank_mask:0xf\n\t" \
    "s_not_b64 vcc, vcc\n\t" \
    "v_and_b32 v100, 0x1fff0, v100\n\t" \
    "v_cndmask_b32_dpp v95, v90, v91, vcc quad_perm:[1,0,3,2] row_mask:0xf bank_mask:0xf\n\t" \
    "v_mul_f32_dpp v98, v101, v113 quad_perm:[0,0,2,2] row_mask:0xf bank_mask:0xf\n\t" \
    "v_mul_f32_dpp v99, v101, v113 quad_perm:[1,1,3,3] row_mask:0xf bank_mask:0xf\n\t" \
    "v_lshlrev_b32 v92, 16, v93\n\t" \
    "v_and_b32 v93, 0xffff0000, v93\n\t" \
    "v_lshlrev_b32 v94, 16, v95\n\t" \
    "v_and_b32 v95, 0xffff0000, v95\n\t" \
    "v_pk_fma_f32 v[92:93], v[92:93], v[98:99], s[78:79] op_sel:[0,0,0] op_sel_hi:[1,0,1]\n\t" \
    "v_pk_fma_f32 v[94:95], v[94:95], v[98:99], s[78:79] op_sel:[0,1,0] op_sel_hi:[1,1,1]\n\t" \
    "v_add_u32_dpp v96, v100, v112 quad_perm:[0,0,2,2] row_mask:0xf bank_mask:0xf\n\t" \
    "v_add_u32_dpp v97, v100, v112 quad_perm:[1,1,3,3] row_mask:0xf bank_mask:0xf\n\t" \
    "v_add_u32 v93, s84, v93\n\t" \
    "v_add_u32 v95, s84, v95\n\t" \
    "v_cmpx_ne_u32 vcc, 0, v98\n\t" \
    "ds_add_u64 v96, v[92:93]\n\t" \
    "s_mov_b64 exec, -1\n\t" \
    "v_cmpx_ne_u32 vcc, 0, v99\n\t" \
    "ds_add_u64 v97, v[94:95]\n\t" \
    "s_mov_b64 exec, -1\n\t" \
    "v_add_u32 v54, v54, v103\n\t" \
    "v_and_b32 v54, -4, v54\n\t" \
    "buffer_load_dwordx3 v[52:54], v54, %5, 0 offen offset:1920 sc1\n\t" \
    "s_bitcmp0_b32 s91, 1\n\t" \
    "s_cbranch_scc1 39f\n\t" \
    "v_add_u32 v58, v58, v103\n\t" \
    "v_and_b32 v58, -4, v58\n\t" \
    "buffer_load_dwordx3 v[56:58], v58, %5, 0 offen offset:2560 sc1\n\t" \
    "39:\n\t" \
    "s_waitcnt lgkmcnt(2)\n\t" \
    "s_cmp_eq_u32 s92, 1\n\t" \
    "s_cbranch_scc1 1f\n\t" \
    "s_barrier\n\t" \
    "s_setprio 3\n\t" \
    "s_waitcnt vmcnt(5)\n\t" \
    "ds_write_b128 v104, v[6:9] offset:0\n\t" \
    "s_waitcnt vmcnt(1)\n\t" \
    "v_bfe_i32 v100, v49, 5, 1\n\t" \
    "v_bfi_b32 v101, v100, s88, v114\n\t" \
    "v_perm_b32 v101, v50, v48, v101\n\t" \
    "v_bfi_b32 v48, v100, v48, v50\n\t" \
    "v_perm_b32 v100, v49, v48, s89\n\t" \
    "v_lshlrev_b32 v100, 3, v100\n\t" \
    "v_bfi_b32 v50, s90, v100, v101\n\t" \
    "v_ashrrev_i32 v101, 3, v20\n\t" \
    "v_lshrrev_b32 v100, 2, v20\n\t" \
    "v_and_b32 v101, 0x87ff0000, v101\n\t" \
    "s_mov_b64 vcc, s[82:83]\n\t" \
    "v_cndmask_b32_dpp v93, v61, v60, vcc quad_perm:[1,0,3,2] row_mask:0xf bank_mask:0xf\n\t" \
    "s_not_b64 vcc, vcc\n\t" \
    "v_and_b32 v100, 0x1fff0, v100\n\t" \
    "v_cndmask_b32_dpp v95, v60, v61, vcc quad_perm:[1,0,3,2] row_mask:0xf bank_mask:0xf\n\t" \
    "v_mul_f32_dpp v98, v101, v113 quad_perm:[0,0,2,2] row_mask:0xf bank_mask:0xf\n\t" \
    "v_mul_f32_dpp v99, v101, v113 quad_perm:[1,1,3,3] row_mask:0xf bank_mask:0xf\n\t" \
    "v_lshlrev_b32 v92, 16, v93\n\t" \
    "v_and_b32 v93, 0xffff0000, v93\n\t" \
    "v_lshlrev_b32 v94, 16, v95\n\t" \
    "v_and_b32 v95, 0xffff0000, v95\n\t" \
    "v_pk_fma_f32 v[92:93], v[92:93], v[98:99], s[78:79] op_sel:[0,0,0] op_sel_hi:[1,0,1]\n\t" \
    "v_pk_fma_f32 v[94:95], v[94:95], v[98:99], s[78:79] op_sel:[0,1,0] op_sel_hi:[1,1,1]\n\t" \
    "v_add_u32_dpp v96, v100, v112 quad_perm:[0,0,2,2] row_mask:0xf bank_mask:0xf\n\t" \
    "v_add_u32_dpp v97, v100, v112 quad_perm:[1,1,3,3] row_mask:0xf bank_mask:0xf\n\t" \
    "v_add_u32 v93, s84, v93\n\t" \
    "v_add_u32 v95, s84, v95\n\t" \
    "ds_add_u64 v96, v[92:93]\n\t" \
    "ds_add_u64 v97, v[94:95]\n\t" \
    "buffer_load_dwordx4 v[6:9], v104, %4, s94 offen\n\t" \
    "ds_write_b128 v104, v[10:13] offset:16384\n\t" \
    "s_setprio 2\n\t" \
    "s_add_u32 s87, s94, 16384\n\t" \
    "buffer_load_dwordx4 v[10:13], v104, %4, s87 offen\n\t" \
    "s_bitcmp0_b32 s91, 0\n\t" \
    "s_cbranch_scc1 40f\n\t" \
    "v_ashrrev_i32 v101, 3, v36\n\t" \
    "v_lshrrev_b32 v100, 2, v36\n\t" \
    "v_and_b32 v101, 0x87ff0000, v101\n\t" \
    "s_mov_b64 vcc, s[82:83]\n\t" \
    "v_cndmask_b32_dpp v93, v77, v76, vcc quad_perm:[1,0,3,2] row_mask:0xf bank_mask:0xf\n\t" \
    "s_not_b64 vcc, vcc\n\t" \
    "v_and_b32 v100, 0x1fff0, v100\n\t" \
    "v_cndmask_b32_dpp v95, v76, v77, vcc quad_perm:[1,0,3,2] row_mask:0xf bank_mask:0xf\n\t" \
    "v_mul_f32_dpp v98, v101, v113 quad_perm:[0,0,2,2] row_mask:0xf bank_mask:0xf\n\t" \
    "v_mul_f32_dpp v99, v101, v113 quad_perm:[1,1,3,3] row_mask:0xf bank_mask:0xf\n\t" \
    "v_lshlrev_b32 v92, 16, v93\n\t" \
    "v_and_b32 v93, 0xffff0000, v93\n\t" \
    "v_lshlrev_b32 v94, 16, v95\n\t" \
    "v_and_b32 v95, 0xffff0000, v95\n\t" \
    "v_pk_fma_f32 v[92:93], v[92:93], v[98:99], s[78:79] op_sel:[0,0,0] op_sel_hi:[1,0,1]\n\t" \
    "v_pk_fma_f32 v[94:95], v[94:95], v[98:99], s[78:79] op_sel:[0,1,0] op_sel_hi:[1,1,1]\n\t" \
    "v_add_u32_dpp v96, v100, v112 quad_perm:[0,0,2,2] row_mask:0xf bank_mask:0xf\n\t" \
    "v_add_u32_dpp v97, v100, v112 quad_perm:[1,1,3,3] row_mask:0xf bank_mask:0xf\n\t" \
    "v_add_u32 v93, s84, v93\n\t" \
    "v_add_u32 v95, s84, v95\n\t" \
    "v_cmpx_ne_u32 vcc, 0, v98\n\t" \
    "ds_add_u64 v96, v[92:93]\n\t" \
    "s_mov_b64 exec, -1\n\t" \
    "v_cmpx_ne_u32 vcc, 0, v99\n\t" \
    "ds_add_u64 v97, v[94:95]\n\t" \
    "s_mov_b64 exec, -1\n\t" \
    "v_ashrrev_i32 v101, 3, v37\n\t" \
    "v_lshrrev_b32 v100, 2, v37\n\t" \
    "v_and_b32 v101, 0x87ff0000, v101\n\t" \
    "s_mov_b64 vcc, s[82:83]\n\t" \
    "v_cndmask_b32_dpp v93, v79, v78, vcc quad_perm:[1,0,3,2] row_mask:0xf bank_mask:0xf\n\t" \
    "s_not_b64 vcc, vcc\n\t" \
    "v_and_b32 v100, 0x1fff0, v100\n\t" \
    "v_cndmask_b32_dpp v95, v78, v79, vcc quad_perm:[1,0,3,2] row_mask:0xf bank_mask:0xf\n\t" \
    "v_mul_f32_dpp v98, v101, v113 quad_perm:[0,0,2,2] row_mask:0xf bank_mask:0xf\n\t" \
    "v_mul_f32_dpp v99, v101, v113 quad_perm:[1,1,3,3] row_mask:0xf bank_mask:0xf\n\t" \
    "v_lshlrev_b32 v92, 16, v93\n\t" \
    "v_and_b32 v93, 0xffff0000, v93\n\t" \
    "v_lshlrev_b32 v94, 16, v95\n\t" \
    "v_and_b32 v95, 0xffff0000, v95\n\t" \
    "v_pk_fma_f32 v[92:93], v[92:93], v[98:99], s[78:79] op_sel:[0,0,0] op_sel_hi:[1,0,1]\n\t" \
    "v_pk_fma_f32 v[94:95], v[94:95], v[98:99], s[78:79] op_sel:[0,1,0] op_sel_hi:[1,1,1]\n\t" \
    "v_add_u32_dpp v96, v100, v112 quad_perm:[0,0,2,2] row_mask:0xf bank_mask:0xf\n\t" \
    "v_add_u32_dpp v97, v100, v112 quad_perm:[1,1,3,3] row_mask:0xf bank_mask:0xf\n\t" \
    "v_add_u32 v93, s84, v93\n\t" \
    "v_add_u32 v95, s84, v95\n\t" \
    "v_cmpx_ne_u32 vcc, 0, v98\n\t" \
    "ds_add_u64 v96, v[92:93]\n\t" \
    "s_mov_b64 exec, -1\n\t" \
    "v_cmpx_ne_u32 vcc, 0, v99\n\t" \
    "ds_add_u64 v97, v[94:95]\n\t" \
    "s_mov_b64 exec, -1\n\t" \
    "40:\n\t" \
    "s_waitcnt vmcnt(2)\n\t" \
    "v_bfe_i32 v100, v53, 5, 1\n\t" \
    "v_bfi_b32 v101, v100, s88, v114\n\t" \
    "v_perm_b32 v101, v54, v52, v101\n\t" \
    "v_bfi_b32 v52, v100, v52, v54\n\t" \
    "v_perm_b32 v100, v53, v52, s89\n\t" \
    "v_lshlrev_b32 v100, 3, v100\n\t" \
    "v_bfi_b32 v54, s90, v100, v101\n\t" \
    "v_ashrrev_i32 v101, 3, v21\n\t" \
    "v_lshrrev_b32 v100, 2, v21\n\t" \
    "v_and_b32 v101, 0x87ff0000, v101\n\t" \
    "s_mov_b64 vcc, s[82:83]\n\t" \
    "v_cndmask_b32_dpp v93, v63, v62, vcc quad_perm:[1,0,3,2] row_mask:0xf bank_mask:0xf\n\t" \
    "s_not_b64 vcc, vcc\n\t" \
    "v_and_b32 v100, 0x1fff0, v100\n\t" \
    "v_cndmask_b32_dpp v95, v62, v63, vcc quad_perm:[1,0,3,2] row_mask:0xf bank_mask:0xf\n\t" \
    "v_mul_f32_dpp v98, v101, v113 quad_perm:[0,0,2,2] row_mask:0xf bank_mask:0xf\n\t" \
    "v_mul_f32_dpp v99, v101, v113 quad_perm:[1,1,3,3] row_mask:0xf bank_mask:0xf\n\t" \
    "v_lshlrev_b32 v92, 16, v93\n\t" \
    "v_and_b32 v93, 0xffff0000, v93\n\t" \
    "v_lshlrev_b32 v94, 16, v95\n\t" \
    "v_and_b32 v95, 0xffff0000, v95\n\t" \
    "v_pk_fma_f32 v[92:93], v[92:93], v[98:99], s[78:79] op_sel:[0,0,0] op_sel_hi:[1,0,1]\n\t" \
    "v_pk_fma_f32 v[94:95], v[94:95], v[98:99], s[78:79] op_sel:[0,1,0] op_sel_hi:[1,1,1]\n\t" \
    "v_add_u32_dpp v96, v100, v112 quad_perm:[0,0,2,2] row_mask:0xf bank_mask:0xf\n\t" \
    "v_add_u32_dpp v97, v100, v112 quad_perm:[1,1,3,3] row_mask:0xf bank_mask:0xf\n\t" \
    "v_add_u32 v93, s84, v93\n\t" \
    "v_add_u32 v95, s84, v95\n\t" \
    "ds_add_u64 v96, v[92:93]\n\t" \
    "ds_add_u64 v97, v[94:95]\n\t" \
    "ds_write_b128 v104, v[14:17] offset:32768\n\t" \
    "s_setprio 1\n\t" \
    "s_add_u32 s87, s94, 32768\n\t" \
    "buffer_load_dwordx4 v[14:17], v104, %4, s87 offen\n\t" \
    "v_and_b32 v23, 0xffff, v42\n\t" \
    "v_lshrrev_b32 v27, 16, v42\n\t" \
    "v_cmp_nlt_f32 s[48:49], |v40|, 2.0\n\t" \
    "v_cmp_nlt_f32 s[50:51], |v41|, 2.0\n\t" \
    "v_and_b32 v31, 0xffff, v46\n\t" \
    "v_lshrrev_b32 v35, 16, v46\n\t" \
    "v_cmp_nlt_f32 s[52:53], |v44|, 2.0\n\t" \
    "v_cmp_nlt_f32 s[54:55], |v45|, 2.0\n\t" \
    "v_and_b32 v39, 0xffff, v50\n\t" \
    "v_lshrrev_b32 v43, 16, v50\n\t" \
    "v_cmp_nlt_f32 s[56:57], |v48|, 2.0\n\t" \
    "v_cmp_nlt_f32 s[58:59], |v49|, 2.0\n\t" \
    "v_and_b32 v47, 0xffff, v54\n\t" \
    "v_lshrrev_b32 v51, 16, v54\n\t" \
    "v_cmp_nlt_f32 s[60:61], |v52|, 2.0\n\t" \
    "v_cmp_nlt_f32 s[62:63], |v53|, 2.0\n\t" \
    "s_bitcmp0_b32 s91, 1\n\t" \
    "s_cbranch_scc1 41f\n\t" \
    "s_waitcnt vmcnt(3)\n\t" \
    "v_bfe_i32 v100, v57, 5, 1\n\t" \
    "v_bfi_b32 v101, v100, s88, v114\n\t" \
    "v_perm_b32 v101, v58, v56, v101\n\t" \
    "v_bfi_b32 v56, v100, v56, v58\n\t" \
    "v_perm_b32 v100, v57, v56, s89\n\t" \
    "v_lshlrev_b32 v100, 3, v100\n\t" \
    "v_bfi_b32 v58, s90, v100, v101\n\t" \
    "v_and_b32 v55, 0xffff, v58\n\t" \
    "v_lshrrev_b32 v59, 16, v58\n\t" \
    "v_cmp_nlt_f32 s[64:65], |v56|, 2.0\n\t" \
    "v_cmp_nlt_f32 s[66:67], |v57|, 2.0\n\t" \
    "41:\n\t" \
    "v_add_u32 v22, v22, v103\n\t" \
    "v_and_b32 v22, -4, v22\n\t" \
    "buffer_load_dwordx3 v[20:22], v22, %5, 0 offen offset:0 sc1\n\t" \
    "s_setprio 0\n\t" \
    "v_ashrrev_i32 v101, 3, v24\n\t" \
    "v_lshrrev_b32 v100, 2, v24\n\t" \
    "v_and_b32 v101, 0x87ff0000, v101\n\t" \
    "s_mov_b64 vcc, s[82:83]\n\t" \
    "v_cndmask_b32_dpp v93, v65, v64, vcc quad_perm:[1,0,3,2] row_mask:0xf bank_mask:0xf\n\t" \
    "s_not_b64 vcc, vcc\n\t" \
    "v_and_b32 v100, 0x1fff0, v100\n\t" \
    "v_cndmask_b32_dpp v95, v64, v65, vcc quad_perm:[1,0,3,2] row_mask:0xf bank_mask:0xf\n\t" \
    "v_mul_f32_dpp v98, v101, v113 quad_perm:[0,0,2,2] row_mask:0xf bank_mask:0xf\n\t" \
    "v_mul_f32_dpp v99, v101, v113 quad_perm:[1,1,3,3] row_mask:0xf bank_mask:0xf\n\t" \
    "v_lshlrev_b32 v92, 16, v93\n\t" \
    "v_and_b32 v93, 0xffff0000, v93\n\t" \
    "v_lshlrev_b32 v94, 16, v95\n\t" \
    "v_and_b32 v95, 0xffff0000, v95\n\t" \
    "v_pk_fma_f32 v[92:93], v[92:93], v[98:99], s[78:79] op_sel:[0,0,0] op_sel_hi:[1,0,1]\n\t" \
    "v_pk_fma_f32 v[94:95], v[94:95], v[98:99], s[78:79] op_sel:[0,1,0] op_sel_hi:[1,1,1]\n\t" \
    "v_add_u32_dpp v96, v100, v112 quad_perm:[0,0,2,2] row_mask:0xf bank_mask:0xf\n\t" \
    "v_add_u32_dpp v97, v100, v112 quad_perm:[1,1,3,3] row_mask:0xf bank_mask:0xf\n\t" \
    "v_add_u32 v93, s84, v93\n\t" \
    "v_add_u32 v95, s84, v95\n\t" \
    "ds_add_u64 v96, v[92:93]\n\t" \
    "ds_add_u64 v97, v[94:95]\n\t" \
    "s_mov_b64 exec, s[80:81]\n\t" \
    "ds_write_b64 v105, v[18:19] offset:49152\n\t" \
    "s_mov_b64 exec, -1\n\t" \
    "s_lshr_b32 s91, s91, 1\n\t" \
    "s_waitcnt lgkmcnt(0)\n\t" \
    "s_sub_u32 s92, s92, 1\n\t" \
    "s_branch 23b\n\t" \
    ".p2align 12\n\t" \
    "kw%c14_4:\n\t" \
    "s_setpc_b64 vcc\n\t" \
    "1:\n\t" \
    "v_add_u32 v100, %9, %3\n\t" \
    "v_cmp_gt_u32 vcc, %10, %3\n\t" \
    "v_bfrev_b32 v101, 16\n\t" \
    "s_nop 0\n\t" \
    "v_cndmask_b32 v100, v101, v100, vcc\n\t" \
    "v_lshlrev_b32 v101, 4, v100\n\t" \
    "buffer_load_dwordx4 v[6:9], v101, %4, 0 offen\n\t" \
    "s_setprio 3\n\t" \
    "v_ashrrev_i32 v101, 3, v20\n\t" \
    "v_lshrrev_b32 v100, 2, v20\n\t" \
    "v_and_b32 v101, 0x87ff0000, v101\n\t" \
    "s_mov_b64 vcc, s[82:83]\n\t" \
    "v_cndmask_b32_dpp v93, v61, v60, vcc quad_perm:[1,0,3,2] row_mask:0xf bank_mask:0xf\n\t" \
    "s_not_b64 vcc, vcc\n\t" \
    "v_and_b32 v100, 0x1fff0, v100\n\t" \
    "v_cndmask_b32_dpp v95, v60, v61, vcc quad_perm:[1,0,3,2] row_mask:0xf bank_mask:0xf\n\t" \
    "v_mul_f32_dpp v98, v101, v113 quad_perm:[0,0,2,2] row_mask:0xf bank_mask:0xf\n\t" \
    "v_mul_f32_dpp v99, v101, v113 quad_perm:[1,1,3,3] row_mask:0xf bank_mask:0xf\n\t" \
    "v_lshlrev_b32 v92, 16, v93\n\t" \
    "v_and_b32 v93, 0xffff0000, v93\n\t" \
    "v_lshlrev_b32 v94, 16, v95\n\t" \
    "v_and_b32 v95, 0xffff0000, v95\n\t" \
    "v_pk_fma_f32 v[92:93], v[92:93], v[98:99], s[78:79] op_sel:[0,0,0] op_sel_hi:[1,0,1]\n\t" \
    "v_pk_fma_f32 v[94:95], v[94:95], v[98:99], s[78:79] op_sel:[0,1,0] op_sel_hi:[1,1,1]\n\t" \
    "v_add_u32_dpp v96, v100, v112 quad_perm:[0,0,2,2] row_mask:0xf bank_mask:0xf\n\t" \
    "v_add_u32_dpp v97, v100, v112 quad_perm:[1,1,3,3] row_mask:0xf bank_mask:0xf\n\t" \
    "v_add_u32 v93, s84, v93\n\t" \
    "v_add_u32 v95, s84, v95\n\t" \
    "ds_add_u64 v96, v[92:93]\n\t" \
    "ds_add_u64 v97, v[94:95]\n\t" \
    "s_setprio 3\n\t" \
    "v_ashrrev_i32 v101, 3, v21\n\t" \
    "v_lshrrev_b32 v100, 2, v21\n\t" \
    "v_and_b32 v101, 0x87ff0000, v101\n\t" \
    "s_mov_b64 vcc, s[82:83]\n\t" \
    "v_cndmask_b32_dpp v93, v63, v62, vcc quad_perm:[1,0,3,2] row_mask:0xf bank_mask:0xf\n\t" \
    "s_not_b64 vcc, vcc\n\t" \
    "v_and_b32 v100, 0x1fff0, v100\n\t" \
    "v_cndmask_b32_dpp v95, v62, v63, vcc quad_perm:[1,0,3,2] row_mask:0xf bank_mask:0xf\n\t" \
    "v_mul_f32_dpp v98, v101, v113 quad_perm:[0,0,2,2] row_mask:0xf bank_mask:0xf\n\t" \
    "v_mul_f32_dpp v99, v101, v113 quad_perm:[1,1,3,3] row_mask:0xf bank_mask:0xf\n\t" \
    "v_lshlrev_b32 v92, 16, v93\n\t" \
    "v_and_b32 v93, 0xffff0000, v93\n\t" \
    "v_lshlrev_b32 v94, 16, v95\n\t" \
    "v_and_b32 v95, 0xffff0000, v95\n\t" \
    "v_pk_fma_f32 v[92:93], v[92:93], v[98:99], s[78:79] op_sel:[0,0,0] op_sel_hi:[1,0,1]\n\t" \
    "v_pk_fma_f32 v[94:95], v[94:95], v[98:99], s[78:79] op_sel:[0,1,0] op_sel_hi:[1,1,1]\n\t" \
    "v_add_u32_dpp v96, v100, v112 quad_perm:[0,0,2,2] row_mask:0xf bank_mask:0xf\n\t" \
    "v_add_u32_dpp v97, v100, v112 quad_perm:[1,1,3,3] row_mask:0xf bank_mask:0xf\n\t" \
    "v_add_u32 v93, s84, v93\n\t" \
    "v_add_u32 v95, s84, v95\n\t" \
    "ds_add_u64 v96, v[92:93]\n\t" \
    "ds_add_u64 v97, v[94:95]\n\t" \
    "s_setprio 2\n\t" \
    "v_ashrrev_i32 v101, 3, v24\n\t" \
    "v_lshrrev_b32 v100, 2, v24\n\t" \
    "v_and_b32 v101, 0x87ff0000, v101\n\t" \
    "s_mov_b64 vcc, s[82:83]\n\t" \
    "v_cndmask_b32_dpp v93, v65, v64, vcc quad_perm:[1,0,3,2] row_mask:0xf bank_mask:0xf\n\t" \
    "s_not_b64 vcc, vcc\n\t" \
    "v_and_b32 v100, 0x1fff0, v100\n\t" \
    "v_cndmask_b32_dpp v95, v64, v65, vcc quad_perm:[1,0,3,2] row_mask:0xf bank_mask:0xf\n\t" \
    "v_mul_f32_dpp v98, v101, v113 quad_perm:[0,0,2,2] row_mask:0xf bank_mask:0xf\n\t" \
    "v_mul_f32_dpp v99, v101, v113 quad_perm:[1,1,3,3] row_mask:0xf bank_mask:0xf\n\t" \
    "v_lshlrev_b32 v92, 16, v93\n\t" \
    "v_and_b32 v93, 0xffff0000, v93\n\t" \
    "v_lshlrev_b32 v94, 16, v95\n\t" \
    "v_and_b32 v95, 0xffff0000, v95\n\t" \
    "v_pk_fma_f32 v[92:93], v[92:93], v[98:99], s[78:79] op_sel:[0,0,0] op_sel_hi:[1,0,1]\n\t" \
    "v_pk_fma_f32 v[94:95], v[94:95], v[98:99], s[78:79] op_sel:[0,1,0] op_sel_hi:[1,1,1]\n\t" \
    "v_add_u32_dpp v96, v100, v112 quad_perm:[0,0,2,2] row_mask:0xf bank_mask:0xf\n\t" \
    "v_add_u32_dpp v97, v100, v112 quad_perm:[1,1,3,3] row_mask:0xf bank_mask:0xf\n\t" \
    "v_add_u32 v93, s84, v93\n\t" \
    "v_add_u32 v95, s84, v95\n\t" \
    "ds_add_u64 v96, v[92:93]\n\t" \
    "ds_add_u64 v97, v[94:95]\n\t" \
    "s_setprio 2\n\t" \
    "v_ashrrev_i32 v101, 3, v25\n\t" \
    "v_lshrrev_b32 v100, 2, v25\n\t" \
    "v_and_b32 v101, 0x87ff0000, v101\n\t" \
    "s_mov_b64 vcc, s[82:83]\n\t" \
    "v_cndmask_b32_dpp v93, v67, v66, vcc quad_perm:[1,0,3,2] row_mask:0xf bank_mask:0xf\n\t" \
    "s_not_b64 vcc, vcc\n\t" \
    "v_and_b32 v100, 0x1fff0, v100\n\t" \
    "v_cndmask_b32_dpp v95, v66, v67, vcc quad_perm:[1,0,3,2] row_mask:0xf bank_mask:0xf\n\t" \
    "v_mul_f32_dpp v98, v101, v113 quad_perm:[0,0,2,2] row_mask:0xf bank_mask:0xf\n\t" \
    "v_mul_f32_dpp v99, v101, v113 quad_perm:[1,1,3,3] row_mask:0xf bank_mask:0xf\n\t" \
    "v_lshlrev_b32 v92, 16, v93\n\t" \
    "v_and_b32 v93, 0xffff0000, v93\n\t" \
    "v_lshlrev_b32 v94, 16, v95\n\t" \
    "v_and_b32 v95, 0xffff0000, v95\n\t" \
    "v_pk_fma_f32 v[92:93], v[92:93], v[98:99], s[78:79] op_sel:[0,0,0] op_sel_hi:[1,0,1]\n\t" \
    "v_pk_fma_f32 v[94:95], v[94:95], v[98:99], s[78:79] op_sel:[0,1,0] op_sel_hi:[1,1,1]\n\t" \
    "v_add_u32_dpp v96, v100, v112 quad_perm:[0,0,2,2] row_mask:0xf bank_mask:0xf\n\t" \
    "v_add_u32_dpp v97, v100, v112 quad_perm:[1,1,3,3] row_mask:0xf bank_mask:0xf\n\t" \
    "v_add_u32 v93, s84, v93\n\t" \
    "v_add_u32 v95, s84, v95\n\t" \
    "v_cmpx_ne_u32 vcc, 0, v98\n\t" \
    "ds_add_u64 v96, v[92:93]\n\t" \
    "s_mov_b64 exec, -1\n\t" \
    "v_cmpx_ne_u32 vcc, 0, v99\n\t" \
    "ds_add_u64 v97, v[94:95]\n\t" \
    "s_mov_b64 exec, -1\n\t" \
    "s_setprio 1\n\t" \
    "v_ashrrev_i32 v101, 3, v28\n\t" \
    "v_lshrrev_b32 v100, 2, v28\n\t" \
    "v_and_b32 v101, 0x87ff0000, v101\n\t" \
    "s_mov_b64 vcc, s[82:83]\n\t" \
    "v_cndmask_b32_dpp v93, v69, v68, vcc quad_perm:[1,0,3,2] row_mask:0xf bank_mask:0xf\n\t" \
    "s_not_b64 vcc, vcc\n\t" \
    "v_and_b32 v100, 0x1fff0, v100\n\t" \
    "v_cndmask_b32_dpp v95, v68, v69, vcc quad_perm:[1,0,3,2] row_mask:0xf bank_mask:0xf\n\t" \
    "v_mul_f32_dpp v98, v101, v113 quad_perm:[0,0,2,2] row_mask:0xf bank_mask:0xf\n\t" \
    "v_mul_f32_dpp v99, v101, v113 quad_perm:[1,1,3,3] row_mask:0xf bank_mask:0xf\n\t" \
    "v_lshlrev_b32 v92, 16, v93\n\t" \
    "v_and_b32 v93, 0xffff0000, v93\n\t" \
    "v_lshlrev_b32 v94, 16, v95\n\t" \
    "v_and_b32 v95, 0xffff0000, v95\n\t" \
    "v_pk_fma_f32 v[92:93], v[92:93], v[98:99], s[78:79] op_sel:[0,0,0] op_sel_hi:[1,0,1]\n\t" \
    "v_pk_fma_f32 v[94:95], v[94:95], v[98:99], s[78:79] op_sel:[0,1,0] op_sel_hi:[1,1,1]\n\t" \
    "v_add_u32_dpp v96, v100, v112 quad_perm:[0,0,2,2] row_mask:0xf bank_mask:0xf\n\t" \
    "v_add_u32_dpp v97, v100, v112 quad_perm:[1,1,3,3] row_mask:0xf bank_mask:0xf\n\t" \
    "v_add_u32 v93, s84, v93\n\t" \
    "v_add_u32 v95, s84, v95\n\t" \
    "v_cmpx_ne_u32 vcc, 0, v98\n\t" \
    "ds_add_u64 v96, v[92:93]\n\t" \
    "s_mov_b64 exec, -1\n\t" \
    "v_cmpx_ne_u32 vcc, 0, v99\n\t" \
    "ds_add_u64 v97, v[94:95]\n\t" \
    "s_mov_b64 exec, -1\n\t" \
    "s_setprio 1\n\t" \
    "v_ashrrev_i32 v101, 3, v29\n\t" \
    "v_lshrrev_b32 v100, 2, v29\n\t" \
    "v_and_b32 v101, 0x87ff0000, v101\n\t" \
    "s_mov_b64 vcc, s[82:83]\n\t" \
    "v_cndmask_b32_dpp v93, v71, v70, vcc quad_perm:[1,0,3,2] row_mask:0xf bank_mask:0xf\n\t" \
    "s_not_b64 vcc, vcc\n\t" \
    "v_and_b32 v100, 0x1fff0, v100\n\t" \
    "v_cndmask_b32_dpp v95, v70, v71, vcc quad_perm:[1,0,3,2] row_mask:0xf bank_mask:0xf\n\t" \
    "v_mul_f32_dpp v98, v101, v113 quad_perm:[0,0,2,2] row_mask:0xf bank_mask:0xf\n\t" \
    "v_mul_f32_dpp v99, v101, v113 quad_perm:[1,1,3,3] row_mask:0xf bank_mask:0xf\n\t" \
    "v_lshlrev_b32 v92, 16, v93\n\t" \
    "v_and_b32 v93, 0xffff0000, v93\n\t" \
    "v_lshlrev_b32 v94, 16, v95\n\t" \
    "v_and_b32 v95, 0xffff0000, v95\n\t" \
    "v_pk_fma_f32 v[92:93], v[92:93], v[98:99], s[78:79] op_sel:[0,0,0] op_sel_hi:[1,0,1]\n\t" \
    "v_pk_fma_f32 v[94:95], v[94:95], v[98:99], s[78:79] op_sel:[0,1,0] op_sel_hi:[1,1,1]\n\t" \
    "v_add_u32_dpp v96, v100, v112 quad_perm:[0,0,2,2] row_mask:0xf bank_mask:0xf\n\t" \
    "v_add_u32_dpp v97, v100, v112 quad_perm:[1,1,3,3] row_mask:0xf bank_mask:0xf\n\t" \
    "v_add_u32 v93, s84, v93\n\t" \
    "v_add_u32 v95, s84, v95\n\t" \
    "v_cmpx_ne_u32 vcc, 0, v98\n\t" \
    "ds_add_u64 v96, v[92:93]\n\t" \
    "s_mov_b64 exec, -1\n\t" \
    "v_cmpx_ne_u32 vcc, 0, v99\n\t" \
    "ds_add_u64 v97, v[94:95]\n\t" \
    "s_mov_b64 exec, -1\n\t" \
    "s_setprio 0\n\t" \
    "v_ashrrev_i32 v101, 3, v32\n\t" \
    "v_lshrrev_b32 v100, 2, v32\n\t" \
    "v_and_b32 v101, 0x87ff0000, v101\n\t" \
    "s_mov_b64 vcc, s[82:83]\n\t" \
    "v_cndmask_b32_dpp v93, v73, v72, vcc quad_perm:[1,0,3,2] row_mask:0xf bank_mask:0xf\n\t" \
    "s_not_b64 vcc, vcc\n\t" \
    "v_and_b32 v100, 0x1fff0, v100\n\t" \
    "v_cndmask_b32_dpp v95, v72, v73, vcc quad_perm:[1,0,3,2] row_mask:0xf bank_mask:0xf\n\t" \
    "v_mul_f32_dpp v98, v101, v113 quad_perm:[0,0,2,2] row_mask:0xf bank_mask:0xf\n\t" \
    "v_mul_f32_dpp v99, v101, v113 quad_perm:[1,1,3,3] row_mask:0xf bank_mask:0xf\n\t" \
    "v_lshlrev_b32 v92, 16, v93\n\t" \
    "v_and_b32 v93, 0xffff0000, v93\n\t" \
    "v_lshlrev_b32 v94, 16, v95\n\t" \
    "v_and_b32 v95, 0xffff0000, v95\n\t" \
    "v_pk_fma_f32 v[92:93], v[92:93], v[98:99], s[78:79] op_sel:[0,0,0] op_sel_hi:[1,0,1]\n\t" \
    "v_pk_fma_f32 v[94:95], v[94:95], v[98:99], s[78:79] op_sel:[0,1,0] op_sel_hi:[1,1,1]\n\t" \
    "v_add_u32_dpp v96, v100, v112 quad_perm:[0,0,2,2] row_mask:0xf bank_mask:0xf\n\t" \
    "v_add_u32_dpp v97, v100, v112 quad_perm:[1,1,3,3] row_mask:0xf bank_mask:0xf\n\t" \
    "v_add_u32 v93, s84, v93\n\t" \
    "v_add_u32 v95, s84, v95\n\t" \
    "v_cmpx_ne_u32 vcc, 0, v98\n\t" \
    "ds_add_u64 v96, v[92:93]\n\t" \
    "s_mov_b64 exec, -1\n\t" \
    "v_cmpx_ne_u32 vcc, 0, v99\n\t" \
    "ds_add_u64 v97, v[94:95]\n\t" \
    "s_mov_b64 exec, -1\n\t" \
    "s_setprio 0\n\t" \
    "v_ashrrev_i32 v101, 3, v33\n\t" \
    "v_lshrrev_b32 v100, 2, v33\n\t" \
    "v_and_b32 v101, 0x87ff0000, v101\n\t" \
    "s_mov_b64 vcc, s[82:83]\n\t" \
    "v_cndmask_b32_dpp v93, v75, v74, vcc quad_perm:[1,0,3,2] row_mask:0xf bank_mask:0xf\n\t" \
    "s_not_b64 vcc, vcc\n\t" \
    "v_and_b32 v100, 0x1fff0, v100\n\t" \
    "v_cndmask_b32_dpp v95, v74, v75, vcc quad_perm:[1,0,3,2] row_mask:0xf bank_mask:0xf\n\t" \
    "v_mul_f32_dpp v98, v101, v113 quad_perm:[0,0,2,2] row_mask:0xf bank_mask:0xf\n\t" \
    "v_mul_f32_dpp v99, v101, v113 quad_perm:[1,1,3,3] row_mask:0xf bank_mask:0xf\n\t" \
    "v_lshlrev_b32 v92, 16, v93\n\t" \
    "v_and_b32 v93, 0xffff0000, v93\n\t" \
    "v_lshlrev_b32 v94, 16, v95\n\t" \
    "v_and_b32 v95, 0xffff0000, v95\n\t" \
    "v_pk_fma_f32 v[92:93], v[92:93], v[98:99], s[78:79] op_sel:[0,0,0] op_sel_hi:[1,0,1]\n\t" \
    "v_pk_fma_f32 v[94:95], v[94:95], v[98:99], s[78:79] op_sel:[0,1,0] op_sel_hi:[1,1,1]\n\t" \
    "v_add_u32_dpp v96, v100, v112 quad_perm:[0,0,2,2] row_mask:0xf bank_mask:0xf\n\t" \
    "v_add_u32_dpp v97, v100, v112 quad_perm:[1,1,3,3] row_mask:0xf bank_mask:0xf\n\t" \
    "v_add_u32 v93, s84, v93\n\t" \
    "v_add_u32 v95, s84, v95\n\t" \
    "v_cmpx_ne_u32 vcc, 0, v98\n\t" \
    "ds_add_u64 v96, v[92:93]\n\t" \
    "s_mov_b64 exec, -1\n\t" \
    "v_cmpx_ne_u32 vcc, 0, v99\n\t" \
    "ds_add_u64 v97, v[94:95]\n\t" \
    "s_mov_b64 exec, -1\n\t" \
    "s_bitcmp0_b32 s91, 0\n\t" \
    "s_cbranch_scc1 42f\n\t" \
    "v_ashrrev_i32 v101, 3, v36\n\t" \
    "v_lshrrev_b32 v100, 2, v36\n\t" \
    "v_and_b32 v101, 0x87ff0000, v101\n\t" \
    "s_mov_b64 vcc, s[82:83]\n\t" \
    "v_cndmask_b32_dpp v93, v77, v76, vcc quad_perm:[1,0,3,2] row_mask:0xf bank_mask:0xf\n\t" \
    "s_not_b64 vcc, vcc\n\t" \
    "v_and_b32 v100, 0x1fff0, v100\n\t" \
    "v_cndmask_b32_dpp v95, v76, v77, vcc quad_perm:[1,0,3,2] row_mask:0xf bank_mask:0xf\n\t" \
    "v_mul_f32_dpp v98, v101, v113 quad_perm:[0,0,2,2] row_mask:0xf bank_mask:0xf\n\t" \
    "v_mul_f32_dpp v99, v101, v113 quad_perm:[1,1,3,3] row_mask:0xf bank_mask:0xf\n\t" \
    "v_lshlrev_b32 v92, 16, v93\n\t" \
    "v_and_b32 v93, 0xffff0000, v93\n\t" \
    "v_lshlrev_b32 v94, 16, v95\n\t" \
    "v_and_b32 v95, 0xffff0000, v95\n\t" \
    "v_pk_fma_f32 v[92:93], v[92:93], v[98:99], s[78:79] op_sel:[0,0,0] op_sel_hi:[1,0,1]\n\t" \
    "v_pk_fma_f32 v[94:95], v[94:95], v[98:99], s[78:79] op_sel:[0,1,0] op_sel_hi:[1,1,1]\n\t" \
    "v_add_u32_dpp v96, v100, v112 quad_perm:[0,0,2,2] row_mask:0xf bank_mask:0xf\n\t" \
    "v_add_u32_dpp v97, v100, v112 quad_perm:[1,1,3,3] row_mask:0xf bank_mask:0xf\n\t" \
    "v_add_u32 v93, s84, v93\n\t" \
    "v_add_u32 v95, s84, v95\n\t" \
    "v_cmpx_ne_u32 vcc, 0, v98\n\t" \
    "ds_add_u64 v96, v[92:93]\n\t" \
    "s_mov_b64 exec, -1\n\t" \
    "v_cmpx_ne_u32 vcc, 0, v99\n\t" \
    "ds_add_u64 v97, v[94:95]\n\t" \
    "s_mov_b64 exec, -1\n\t" \
    "v_ashrrev_i32 v101, 3, v37\n\t" \
    "v_lshrrev_b32 v100, 2, v37\n\t" \
    "v_and_b32 v101, 0x87ff0000, v101\n\t" \
    "s_mov_b64 vcc, s[82:83]\n\t" \
    "v_cndmask_b32_dpp v93, v79, v78, vcc quad_perm:[1,0,3,2] row_mask:0xf bank_mask:0xf\n\t" \
    "s_not_b64 vcc, vcc\n\t" \
    "v_and_b32 v100, 0x1fff0, v100\n\t" \
    "v_cndmask_b32_dpp v95, v78, v79, vcc quad_perm:[1,0,3,2] row_mask:0xf bank_mask:0xf\n\t" \
    "v_mul_f32_dpp v98, v101, v113 quad_perm:[0,0,2,2] row_mask:0xf bank_mask:0xf\n\t" \
    "v_mul_f32_dpp v99, v101, v113 quad_perm:[1,1,3,3] row_mask:0xf bank_mask:0xf\n\t" \
    "v_lshlrev_b32 v92, 16, v93\n\t" \
    "v_and_b32 v93, 0xffff0000, v93\n\t" \
    "v_lshlrev_b32 v94, 16, v95\n\t" \
    "v_and_b32 v95, 0xffff0000, v95\n\t" \
    "v_pk_fma_f32 v[92:93], v[92:93], v[98:99], s[78:79] op_sel:[0,0,0] op_sel_hi:[1,0,1]\n\t" \
    "v_pk_fma_f32 v[94:95], v[94:95], v[98:99], s[78:79] op_sel:[0,1,0] op_sel_hi:[1,1,1]\n\t" \
    "v_add_u32_dpp v96, v100, v112 quad_perm:[0,0,2,2] row_mask:0xf bank_mask:0xf\n\t" \
    "v_add_u32_dpp v97, v100, v112 quad_perm:[1,1,3,3] row_mask:0xf bank_mask:0xf\n\t" \
    "v_add_u32 v93, s84, v93\n\t" \
    "v_add_u32 v95, s84, v95\n\t" \
    "v_cmpx_ne_u32 vcc, 0, v98\n\t" \
    "ds_add_u64 v96, v[92:93]\n\t" \
    "s_mov_b64 exec, -1\n\t" \
    "v_cmpx_ne_u32 vcc, 0, v99\n\t" \
    "ds_add_u64 v97, v[94:95]\n\t" \
    "s_mov_b64 exec, -1\n\t" \
    "42:\n\t" \
    "s_setprio 0\n\t" \
    "s_branch 8f\n\t" \
    "9:\n\t" \
    "s_waitcnt vmcnt(0)\n\t" \
    "v_add_u32 v100, %9, %3\n\t" \
    "v_cmp_gt_u32 vcc, %10, %3\n\t" \
    "v_bfrev_b32 v101, 16\n\t" \
    "s_nop 0\n\t" \
    "v_cndmask_b32 v100, v101, v100, vcc\n\t" \
    "v_lshlrev_b32 v101, 4, v100\n\t" \
    "buffer_load_dwordx4 v[6:9], v101, %4, 0 offen\n\t" \
    "8:\n\t" \
    "s_waitcnt vmcnt(0) lgkmcnt(0)\n\t" \
    ""
#define K2_ASM_CLOBBERS \
    "v10", "v11", "v12", "v13", "v14", "v15", "v16", "v17", "v18", "v19", "v20", "v21", "v22", "v23", "v24", "v25", "v26", "v27", "v28", "v29", "v30", "v31", "v32", "v33", "v34", "v35", "v36", "v37", "v38", "v39", "v40", "v41", "v42", "v43", "v44", "v45", "v46", "v47", "v48", "v49", "v50", "v51", "v52", "v53", "v54", "v55", "v56", "v57", "v58", "v59", "v60", "v61", "v62", "v63", "v64", "v65", "v66", "v67", "v68", "v69", "v70", "v71", "v72", "v73", "v74", "v75", "v76", "v77", "v78", "v79", "v80", "v81", "v82", "v83", "v84", "v85", "v86", "v87", "v88", "v89", "v90", "v91", "v92", "v93", "v94", "v95", "v96", "v97", "v98", "v99", "v100", "v101", "v102", "v103", "v104", "v105", "v106", "v107", "v108", "v109", "v110", "v111", "v112", "v113", "v114", \
    "s48", "s49", "s50", "s51", "s52", "s53", "s54", "s55", "s56", "s57", "s58", "s59", "s60", "s61", "s62", "s63", "s64", "s65", "s66", "s67", "s68", "s69", "s70", "s71", "s72", "s73", "s74", "s75", "s76", "s77", "s78", "s79", "s80", "s81", "s82", "s83", "s84", "s85", "s86", "s87", "s88", "s89", "s90", "s91", "s92", "s93", "s94", "s95"

DEVI u32x4 raw_rsrc(const void* p, u32 bytes) {
    u64 a = (u64)p;
    return (u32x4){(u32)a, (u32)(a >> 32) & 0xffffu, bytes, 0x00020000u};
}

template <bool FUSED>
DEVI void acc_phase(const Args& A, const u32 mx, Rbf rbf) {
    const u32 E = A.E, nchunks = FUSED ? 2 * NWG : A.nchunks;
    const unsigned char* const rec = A.ws + WS_REC;
    const u32* const offs = A.offs; const u32x2* const m2 = A.m2;
    const u32 wave = __builtin_amdgcn_readfirstlane(threadIdx.x >> 6);
    const u32 b = blockIdx.x;
    const u32 bucket = (b & 7) * 32 + (b >> 3);
    const u32 g = bucket >> 6, o = bucket & 63;

    const u32 q0 = g * QP, qn = (g == 3 ? NP - 3 * QP : QP);
    u32x4 mrow = {0u, 0u, 0u, 0u};
    u32 total = 0;
    const u32 ex = mx >> 7;
    bool slow = ex > 249;
    const u32 K = ((ex + 5) << 23) | 0x400000u;
    for (u32 kb = 0; kb < nchunks; kb += 512) {
        const u32 nk = nchunks - kb < 512 ? nchunks - kb : 512;
        u32 tot, longest;
        if (!FUSED) {
            total = opaque(total);
            const u16 wf = load_wfrag(A, opaque(threadIdx.x));
            lds_barrier();
            prep_lds(wf, opaque(threadIdx.x), kb == 0);
            if (threadIdx.x == 0) *LDSP(u32, K2_MISC + 64) = 1;
        }
        asm volatile("s_nop 4\n\t" K2_ASM_TEXT
                     : "=&v"(tot), "=&s"(longest), "=&{v[6:9]}"(mrow)
                     : "v"(threadIdx.x), "s"(raw_rsrc(m2, E * 8)),
                       "s"(raw_rsrc(rec + (size_t)kb * CHB, nk * CHB)), "s"(__builtin_amdgcn_readfirstlane(slow ? 0u : K)),
                       "s"(g * (2 * NST * RI * 8)), "s"(wave),
                       "s"(o * NP + q0), "s"(qn), "s"(A.ws), "s"(FUSED ? b : 256u),
                       "s"(offs + (size_t)(kb >> 1) * OFF_STRIDE + bucket * 16), "i"(FUSED ? 1 : 0)
                     : "memory", "vcc", "scc", K2_ASM_CLOBBERS);
        total += tot;
        if (FUSED && __builtin_amdgcn_readfirstlane(*LDSP(u32, K2_MISC + 64)) == 0) return;
        if (slow) break;
        if (__builtin_expect(longest > 320, 0)) {
            const u32 lane = opaque(threadIdx.x) & 63;
            const rsrc_t rs_m = make_rsrc(m2, E * 8);
            const rsrc_t rs_rec = make_rsrc(rec + (size_t)kb * CHB, nk * CHB);
            const u32 xa = K2_EXP + wave * 320;
            *LDSP(u32, xa + (lane & 15) * 4) = 0;
            const u32 kl = (lane >> 1) * 16 + wave + (lane & 1) * NWG;
            const float magic = __uint_as_float(K);
            const u32 adj = (1u << 26) - K;
#pragma unroll 1
            for (u32 st = 0; st < NST; st++) {
                const u32 ent = lane < 32 ? ld_agent(offs + tab_index(kb + kl, bucket, st)) : 0u;
                const u32 o0 = (ent & 0xffffu) >> 1, n = ((ent >> 16) + 1) >> 1;
                const u32 incl = half_incl_add(n);
                const u32 start = incl - n;
                const u32 N = __builtin_amdgcn_readlane(incl, 31);
                for (u32 skip = 320; skip < N; skip += 512) {
                    const bool live = incl > skip && n != 0;
                    const u32 rank = __builtin_amdgcn_mbcnt_lo((u32)__ballot(live), 0u);
                    const u32 bit = start - skip - 1;
                    CBAR();
                    if (live) {
                        *LDSP(u32, xa + 64 + rank * 4) = kl * CHB + (o0 - start + skip) * 10;
                        if (bit < 512) __hip_atomic_fetch_or(LDSP(u32, xa + (bit >> 5) * 4), 1u << (bit & 31), __ATOMIC_RELAXED, __HIP_MEMORY_SCOPE_WORKGROUP);
                    }
                    CBAR();
                    const u32 bw = *LDSP(u32, xa + (lane & 15) * 4);
                    CBAR();
                    *LDSP(u32, xa + (lane & 15) * 4) = 0;
                    u32 pre = xa + 64;
#pragma unroll 1
                    for (u32 s = 0; s < 8; s++) {
                        u32 lo = __builtin_amdgcn_readlane(bw, 2 * s), hi = __builtin_amdgcn_readlane(bw, 2 * s + 1);
                        u32 ri = __builtin_amdgcn_mbcnt_hi(hi, __builtin_amdgcn_mbcnt_lo(lo, 0u));
                        u32 fq = *LDSP(u32, pre + ri * 4);
                        pre += __builtin_popcountll(((u64)hi << 32) | lo) * 4;
                        if (skip + s * 64 + lane < N) {
                            const u32 at = fq + (s * 64 + lane) * 10;
                            const u32x3 rq = __builtin_amdgcn_raw_buffer_load_b96(rs_rec, at & ~3u, 0, AUX_SC1);
                            const bool hi = at & 2;
#pragma unroll
                            for (int h = 0; h < 2; h++) {
                                const u32 w = h ? (hi ? rq.z : rq.y) : (hi ? rq.y : rq.x);
                                float cf; u32 row1, il;
                                unpack_rec(w, ((hi ? rq.x >> 16 : rq.z) >> (8 * h)) & 0xffu, cf, row1, il);
                                if (row1 == 0) continue;
                                u32x2 mv = __builtin_amdgcn_raw_buffer_load_b64(
                                    rs_m, (((g * NST + st) * 2 + ((w >> 30) & 1)) * RI + il) * 8, 0, 0);
                                f32x2 c2 = {cf, cf}, mg = {magic, magic};
                                f32x2 y01 = __builtin_elementwise_fma(unpack_bf16(mv.x), c2, mg);
                                f32x2 y23 = __builtin_elementwise_fma(unpack_bf16(mv.y), c2, mg);
                                u64 A = ((u64)(__float_as_uint(y01.y) + adj) << 32) | __float_as_uint(y01.x);
                                u64 B = ((u64)(__float_as_uint(y23.y) + adj) << 32) | __float_as_uint(y23.x);
                                u32 oa = K2_ACC0 + row1 * 16;
                                __hip_atomic_fetch_add(LDSP(u64, oa), A, __ATOMIC_RELAXED, __HIP_MEMORY_SCOPE_WORKGROUP);
                                __hip_atomic_fetch_add(LDSP(u64, oa + 8), B, __ATOMIC_RELAXED, __HIP_MEMORY_SCOPE_WORKGROUP);
                            }
                        }
                    }
                }
            }
        }
    }
    const u32 tid = opaque(threadIdx.x), lane = tid & 63;
    if (!FUSED) {
        const rsrc_t rs_d = make_rsrc(A.dist, E * 2);
        rbf.r0 = rbf_row(bf2f(__builtin_amdgcn_raw_buffer_load_b16(rs_d, own_pair(tid) * 4, 0, 0)), (cptr)A.centers);
        rbf.r1 = rbf_row(bf2f(__builtin_amdgcn_raw_buffer_load_b16(rs_d, own_pair(tid) * 4 + 2, 0, 0)), (cptr)A.centers);
    }
    const cptr w_down = (cptr)A.w_down, w_up = (cptr)A.w_up;
    unsigned char* const part = A.ws + WS_PART;
    const u32x4 wfrag = *LDSP(u32x4, K2_WFR + lane * 16);
    const bool mine = tid < qn;
    const u32 gp = o * NP + q0 + tid;
    lds_barrier();
    unsigned char* const slab16 = part + (size_t)o * 4 * (RO * 8);
    unsigned char* const slab32 = part + (size_t)NWG * (RO * 8) + (size_t)o * 4 * (RO * 16);
    const int sc = (int)ex - 145;
    const u32 k10 = K >> 22;
    auto unfix = [&](u32 al, u32 ah, u32 bl, u32 bh, u32& cnt) __attribute__((always_inline)) -> f32x4 {
        const u32 n = (ah + (1u << 25)) >> 26;
        cnt += n;
        const u32 nk = __umul24(n, k10);
        const u32 lo = nk << 22, hi = nk >> 10;
        const int s0 = (int)(al - lo), s2 = (int)(bl - lo);
        const int s1 = __builtin_amdgcn_sbfe((int)ah, 0, 26) - (int)hi;
        const int s3 = __builtin_amdgcn_sbfe((int)bh, 0, 26) - (int)hi;
        return (f32x4){(float)s0, (float)s1, (float)s2, (float)s3} * 0x1p-10f;
    };
    auto fixed_pair = [&](u32 i, u32& cnt, f32x4& r0, f32x4& r1) __attribute__((always_inline)) {
        u32x4 x = *LDSP(u32x4, K2_ACCA + i * 32);
        u32x4 y = *LDSP(u32x4, K2_ACCA + i * 32 + 16);
        r0 = unfix(x.x, x.y, x.z, x.w, cnt);
        r1 = unfix(y.x, y.y, y.z, y.w, cnt);
    };
    typedef __attribute__((ext_vector_type(4))) _Float16 f16x4;
    u32x4 ox = {0u, 0u, 0u, 0u}, oy = ox;
    if (!slow) {
        const rsrc_t rs_out = make_rsrc(slab16 + g * (RO * 8), RO * 8);
        u32 cnt = 0;
#pragma unroll
        for (int j = 0; j < 3; j++) {
            const u32 k = tid + j * 1024;
            if (k < NP - qn) {
                u32 i = q0 + qn + k;
                i = i >= NP ? i - NP : i;
                f32x4 r0, r1;
                fixed_pair(i, cnt, r0, r1);
                u32x2 h0 = __builtin_bit_cast(u32x2, __builtin_convertvector(r0, f16x4));
                u32x2 h1 = __builtin_bit_cast(u32x2, __builtin_convertvector(r1, f16x4));
                __builtin_amdgcn_raw_buffer_store_b128((u32x4){h0.x, h0.y, h1.x, h1.y}, rs_out, i * 16, 0, AUX_SC1);
            }
        }
        if (mine) {
            ox = *LDSP(u32x4, K2_ACCA + (q0 + tid) * 32); oy = *LDSP(u32x4, K2_ACCA + (q0 + tid) * 32 + 16);
            cnt += ((ox.y + (1u << 25)) >> 26) + ((oy.y + (1u << 25)) >> 26);
        }
        cnt = wave_incl_add(cnt - total);
        if (lane == 63) *LDSP(u32, K2_MISC + wave * 4) = cnt;
    }
    typedef __attribute__((ext_vector_type(8))) __bf16 bf16x8;
    typedef __attribute__((ext_vector_type(16))) float f32x16;
    auto gates = [&](u32x4 rb) __attribute__((always_inline)) -> f32x16 {
        f32x16 z = {0.f, 0.f, 0.f, 0.f, 0.f, 0.f, 0.f, 0.f, 0.f, 0.f, 0.f, 0.f, 0.f, 0.f, 0.f, 0.f};
        const u32x4 zero = {0u, 0u, 0u, 0u};
        const bool nan_any = __any(((rb.x | rb.y | rb.z | rb.w) & 0x40004000u) != 0);
        if (__builtin_expect(!nan_any, 1))
            return __builtin_amdgcn_mfma_f32_32x32x16_bf16(__builtin_bit_cast(bf16x8, wfrag), __builtin_bit_cast(bf16x8, rb), z, 0, 0, 0);
        f32x16 lo = __builtin_amdgcn_mfma_f32_32x32x16_bf16(__builtin_bit_cast(bf16x8, wfrag), __builtin_bit_cast(bf16x8, lane < 32 ? rb : zero), z, 0, 0, 0);
        f32x16 hi = __builtin_amdgcn_mfma_f32_32x32x16_bf16(__builtin_bit_cast(bf16x8, wfrag), __builtin_bit_cast(bf16x8, lane < 32 ? zero : rb), z, 0, 0, 0);
        return lane < 32 ? lo : hi;
    };
    f32x16 gt0, gt1;
    const bool busy = wave * 64 < qn;
    if (FUSED) NEW_PAGE(0);
    asm volatile("s_waitcnt vmcnt(0) lgkmcnt(0)\n\ts_barrier" ::: "memory");
    if (!slow) {
        u32 d = *LDSP(u32, K2_MISC + (lane & 15) * 4);
        d += dpp0<0x111, 0xf, 0xf>(d); d += dpp0<0x112, 0xf, 0xf>(d); d += dpp0<0x114, 0xf, 0xf>(d); d += dpp0<0x118, 0xf, 0xf>(d);
        slow = __builtin_amdgcn_readlane(d, 15) != 0;
    }
    if (__builtin_expect(slow, 0)) {
        k2_slow(A, bucket, g);
        const rsrc_t rs_out = make_rsrc(slab32 + g * (RO * 16), RO * 16);
        for (u32 i = tid; i < RO; i += 1024)
            __builtin_amdgcn_raw_buffer_store_b128(*LDSP(u32x4, K2_ACCA + i * 16), rs_out, i * 16, 0, AUX_SC1);
        ox = oy = (u32x4){0u, 0u, 0u, 0u};
        asm volatile("s_waitcnt vmcnt(0)\n\ts_barrier" ::: "memory");
    }
    u32* flag = (u32*)(A.ws + WS_ARR) + o * 64;
    if (wave == 15) {
        u32 seen = 0;
        if (lane == 0) {
            seen = __hip_atomic_fetch_add(flag, 1u + (slow ? 256u << g : 0u), __ATOMIC_RELAXED, __HIP_MEMORY_SCOPE_AGENT);
            seen += 1u + (slow ? 256u << g : 0u);
            for (u32 spin = 0; (seen & 255u) < 4u && spin < (1u << 23); spin++) {
                __builtin_amdgcn_s_sleep(1);
                seen = __hip_atomic_load(flag, __ATOMIC_RELAXED, __HIP_MEMORY_SCOPE_AGENT);
            }
            *LDSP(u32, K2_MISC + 68) = seen;
        }
    }
    asm volatile("" : "+v"(ox), "+v"(oy));
    u32 unused = 0;
    f32x4 own0 = unfix(ox.x, ox.y, ox.z, ox.w, unused), own1 = unfix(oy.x, oy.y, oy.z, oy.w, unused);
    asm volatile("" : "+v"(own0), "+v"(own1));
    if (busy) { gt0 = gates(rbf.r0); gt1 = gates(rbf.r1); }
    lds_barrier();
    const u32 fl = __builtin_amdgcn_readfirstlane(*LDSP(u32, K2_MISC + 68)) >> 8;
    if (mine) {
        const rsrc_t rs_in = make_rsrc(slab16, 4 * RO * 8);
        u32x4 p[3];
#pragma unroll
        for (int t = 0; t < 3; t++) {
            const u32 src = (g + 1 + t) & 3;
            p[t] = __builtin_amdgcn_raw_buffer_load_b128(rs_in, ((fl >> src) & 1) ? OOB : (src * NP + q0 + tid) * 16, 0, AUX_SC1);
        }
#pragma unroll
        for (int t = 0; t < 3; t++) {
            own0 += __builtin_convertvector(__builtin_bit_cast(f16x4, (u32x2){p[t].x, p[t].y}), f32x4);
            own1 += __builtin_convertvector(__builtin_bit_cast(f16x4, (u32x2){p[t].z, p[t].w}), f32x4);
        }
        f32x4 sv[2];
        sv[0] = (f32x4){__builtin_ldexpf(own0.x, sc + 10), __builtin_ldexpf(own0.y, sc + 10), __builtin_ldexpf(own0.z, sc + 10), __builtin_ldexpf(own0.w, sc + 10)};
        sv[1] = (f32x4){__builtin_ldexpf(own1.x, sc + 10), __builtin_ldexpf(own1.y, sc + 10), __builtin_ldexpf(own1.z, sc + 10), __builtin_ldexpf(own1.w, sc + 10)};
        if (__builtin_expect(fl != 0, 0)) {
            const rsrc_t rs_f = make_rsrc(slab32, 4 * RO * 16);
            for (u32 src = 0; src < 4; src++)
                if ((fl >> src) & 1) {
                    sv[0] += __builtin_bit_cast(f32x4, __builtin_amdgcn_raw_buffer_load_b128(rs_f, (src * NP + q0 + tid) * 32, 0, AUX_SC1));
                    sv[1] += __builtin_bit_cast(f32x4, __builtin_amdgcn_raw_buffer_load_b128(rs_f, (src * NP + q0 + tid) * 32 + 16, 0, AUX_SC1));
                }
        }
        u32 res[4];
#pragma unroll
        for (int j = 0; j < 2; j++) {
            f32x2 a[2], u[2];
            const f32x16 gt = j ? gt1 : gt0;
#pragma unroll
            for (int k = 0; k < 2; k++)
                a[k] = (unpack_bf16(w_down[k]) * sv[j].x + unpack_bf16(w_down[2 + k]) * sv[j].y + unpack_bf16(w_down[4 + k]) * sv[j].z +
                        unpack_bf16(w_down[6 + k]) * sv[j].w) * (f32x2){gt[2 * k], gt[2 * k + 1]};
#pragma unroll
            for (int k = 0; k < 2; k++)
                u[k] = unpack_bf16(w_up[k]) * a[0].x + unpack_bf16(w_up[2 + k]) * a[0].y + unpack_bf16(w_up[4 + k]) * a[1].x +
                       unpack_bf16(w_up[6 + k]) * a[1].y;
            res[2 * j] = cvt_pk_bf16(unpack_bf16(j ? mrow.z : mrow.x) + u[0] * (f32x2){gt[4], gt[5]});
            res[2 * j + 1] = cvt_pk_bf16(unpack_bf16(j ? mrow.w : mrow.y) + u[1] * (f32x2){gt[6], gt[7]});
        }
        __builtin_amdgcn_raw_buffer_store_b128((u32x4){res[0], res[1], res[2], res[3]}, make_rsrc(A.out, E * 8), gp * 16, 0, 0);
    }
}

__global__ void __launch_bounds__(1024) k1_fused(const Args A) {
    Rbf rbf;
    const u32 mx = sort_phase<true>(A, rbf);
    if (mx >> 31) { *LDSP(u32, K2_MISC + 64) = 0; return; }
    acc_phase<true>(A, mx, rbf);
}
__global__ void __launch_bounds__(1024) k1_sort(const Args A) { Rbf rbf; sort_phase<false>(A, rbf); }
__global__ void __launch_bounds__(1024) k2_acc(const Args A) {
    const u32x4 mm = ((const u32x4*)(A.ws + WS_FLAG))[threadIdx.x & 63];
    acc_phase<false>(A, __builtin_amdgcn_readlane(wave_incl_max(umax(umax(mm.x, mm.y), umax(mm.z, mm.w))), 63), Rbf{});
}

DEVI float rbf_any(float d, float c) { float x = rbf16(d - c); x = rbf16(x * x); x = rbf16(-10.0f * x); return rbf16(__expf(x)); }
__global__ void any_scatter(const u16* m, const int* idx_in, const int* idx_out, const u16* dist, const u16* angle,
                            const u16* centers, const u16* freq, const u16* w_sbf, const u16* w_down, float* agg,
                            u32 T, u32 De, u32 NR, u32 L) {
    const size_t i = (size_t)blockIdx.x * blockDim.x + threadIdx.x;
    if (i >= (size_t)T * De) return;
    const u32 t = (u32)(i / De), k = (u32)(i % De);
    const u32 ei = (u32)idx_in[t], eo = (u32)idx_out[t];
    float md = 0.f, gate = 0.f;
    for (u32 j = 0; j < De; j++) md += bf2f(m[(size_t)ei * De + j]) * bf2f(w_down[j * De + k]);
    for (u32 l = 0; l < L; l++) {
        const float x = rbf16(bf2f(freq[l]) * bf2f(angle[t]));
        const float cs = rbf16(__builtin_amdgcn_cosf(rev_big(x)));
        for (u32 r = 0; r < NR; r++) gate += rbf_any(bf2f(dist[eo]), bf2f(centers[r])) * cs * bf2f(w_sbf[(r * L + l) * De + k]);
    }
    atomicAdd(agg + (size_t)eo * De + k, md * gate);
}
__global__ void any_final(const u16* m, const u16* dist, const u16* centers, const u16* w_rbf, const u16* w_up,
                          const float* agg, u16* out, u32 E, u32 De, u32 NR) {
    const size_t i = (size_t)blockIdx.x * blockDim.x + threadIdx.x;
    if (i >= (size_t)E * De) return;
    const u32 e = (u32)(i / De), k = (u32)(i % De);
    float up = 0.f, gate = 0.f;
    for (u32 j = 0; j < De; j++) up += agg[(size_t)e * De + j] * bf2f(w_up[j * De + k]);
    for (u32 r = 0; r < NR; r++) gate += rbf_any(bf2f(dist[e]), bf2f(centers[r])) * bf2f(w_rbf[r * De + k]);
    out[i] = (u16)cvt_pk_bf16(bf2f(m[i]) + up * gate, 0.f);
}

extern "C" void kernel_launch(void* const* d_in, const int* in_sizes, int n_in,
                              void* d_out, int out_size, void* d_ws, size_t ws_size,
                              hipStream_t stream) {
    const u32 E = (u32)in_sizes[3];
    const u32 T = (u32)in_sizes[1];
    u32 CH = (T + 2 * NWG - 1) / (2 * NWG); CH = (CH + 7) & ~7u;
    if (CH > 25000u) CH = 25000u;
    if (CH < 8) CH = 8;
    const u32 nchunks = T ? (T + CH - 1) / CH : 1;
    const bool full = (T % CH == 0) && CH >= QFULL * 4 && nchunks == 2u * NWG;

    const u32 De = E ? (u32)in_sizes[0] / E : 0, NR = (u32)in_sizes[5], L = (u32)in_sizes[6];
    const size_t need = WS_REC + (size_t)nchunks * CHB + (size_t)(nchunks + 511) / 512 * NWG * OFF_STRIDE * 4;
    if (De != 4 || NR != 8 || L != 1 || E > 64 * RO || need > ws_size) {
        if (E == 0 || De == 0) return;
        float* agg = (float*)d_ws;
        (void)hipMemsetAsync(agg, 0, (size_t)E * De * 4, stream);
        if (T)
            any_scatter<<<(u32)(((size_t)T * De + 255) / 256), 256, 0, stream>>>(
                (const u16*)d_in[0], (const int*)d_in[1], (const int*)d_in[2], (const u16*)d_in[3], (const u16*)d_in[4],
                (const u16*)d_in[5], (const u16*)d_in[6], (const u16*)d_in[8], (const u16*)d_in[9], agg, T, De, NR, L);
        any_final<<<(u32)(((size_t)E * De + 255) / 256), 256, 0, stream>>>(
            (const u16*)d_in[0], (const u16*)d_in[3], (const u16*)d_in[5], (const u16*)d_in[7], (const u16*)d_in[10], agg,
            (u16*)d_out, E, De, NR);
        return;
    }

    Args A;
    A.idx_in = (const int*)d_in[1]; A.idx_out = (const int*)d_in[2]; A.angle = (const u16*)d_in[4];
    A.CH = CH; A.E = E;
    A.freq = (const u16*)d_in[6]; A.m2 = (const u32x2*)d_in[0]; A.ws = (unsigned char*)d_ws;
    A.offs = (u32*)(A.ws + WS_REC + (size_t)nchunks * CHB);
    A.dist = (const u16*)d_in[3]; A.centers = (const u32*)d_in[5];
    A.w_down = (const u32*)d_in[9]; A.w_up = (const u32*)d_in[10];
    A.out = (u32x2*)d_out; A.w_sbf = (const u16*)d_in[8]; A.w_rbf = (const u16*)d_in[7];
    A.T = T; A.nchunks = nchunks;
    if (full) {
        (void)hipFuncSetAttribute((const void*)k1_fused, hipFuncAttributeMaxDynamicSharedMemorySize, K1_LDS);
        k1_fused<<<NWG, 1024, K1_LDS, stream>>>(A);
    } else {
        (void)hipFuncSetAttribute((const void*)k1_sort, hipFuncAttributeMaxDynamicSharedMemorySize, K1_LDS);
        k1_sort<<<NWG, 1024, K1_LDS, stream>>>(A);
        (void)hipFuncSetAttribute((const void*)k2_acc, hipFuncAttributeMaxDynamicSharedMemorySize, K1_LDS);
        k2_acc<<<NWG, 1024, K1_LDS, stream>>>(A);
    }
}
```
